```python
import torch

def _softmax(x, axis=-1):
    return torch.softmax(x, dim=axis)

def _gelu(x):
    return torch.nn.functional.gelu(x, approximate='tanh')

def _silu(x):
    return torch.nn.functional.silu(x)

def _sigmoid(x):
    return torch.sigmoid(x)

def _layernorm(x, axis=-1, eps=1e-05):
    m = x.mean(dim=axis, keepdim=True)
    v = x.var(dim=axis, keepdim=True, unbiased=False)
    return (x - m) / torch.sqrt(v + eps)

def _scatter_add(tgt, idx, src, axis=0):
    return tgt.clone().index_add_(axis, idx, src)

def _segment_sum(src, seg, n):
    out = torch.zeros((n,) + src.shape[1:], dtype=src.dtype)
    return out.index_add_(0, seg, src)

def _pad(x, pw):
    flat = tuple((v for pair in reversed(pw) for v in pair))
    return torch.nn.functional.pad(x, flat)
kernel_name = 'adaLN_cond__memory_aug_recurrent_mem_tr__glu_gated_geglu__prompt'

def setup_inputs():

    _w = lambda a: (lambda t: t.to(torch.bfloat16) if t.is_floating_point() else t)(torch.as_tensor(a))

    def _A_setup():
        B, S, Dm = (1, 2048, 512)
        H, Dh = (32, 128)
        M, seg = (8, 32)
        seg = min(seg, S)
        S = S // seg * seg
        return {'x': _w((lambda *s: torch.randn(*s, dtype=torch.bfloat16) * 0.05)(B, S, Dm)), 'mem0': _w((lambda *s: torch.randn(*s, dtype=torch.bfloat16) * 0.8)(B, M, Dm)), 'Wq': _w((lambda *s: torch.randn(*s, dtype=torch.bfloat16) * 0.8)(Dm, H, Dh) * Dm ** (-0.5)), 'Wk': _w((lambda *s: torch.randn(*s, dtype=torch.bfloat16) * 0.8)(Dm, H, Dh) * Dm ** (-0.5)), 'Wv': _w((lambda *s: torch.randn(*s, dtype=torch.bfloat16) * 0.8)(Dm, H, Dh) * Dm ** (-0.5)), 'Wo': _w((lambda *s: torch.randn(*s, dtype=torch.bfloat16) * 0.8)(H, Dh, Dm) * (H * Dh) ** (-0.5)), 'seg_len': seg}
    _A_in = _A_setup()

    def _B_setup():
        B, S, Dm, Df = (1, 2048, 512, 1376)
        return {'x': _w((lambda *s: torch.randn(*s, dtype=torch.bfloat16) * 0.05)(B, S, Dm)), 'w_gate': _w((lambda *s: torch.randn(*s, dtype=torch.bfloat16) * 0.4)(Dm, Df) * Dm ** (-0.5)), 'w_up': _w((lambda *s: torch.randn(*s, dtype=torch.bfloat16) * 0.4)(Dm, Df) * Dm ** (-0.5)), 'w_down': _w((lambda *s: torch.randn(*s, dtype=torch.bfloat16) * 0.4)(Df, Dm) * Df ** (-0.5))}
    _B_in = _B_setup()
    return {'A_x': _A_in['x'], 'A_mem0': _A_in['mem0'], 'A_Wq': _A_in['Wq'], 'A_Wk': _A_in['Wk'], 'A_Wv': _A_in['Wv'], 'A_Wo': _A_in['Wo'], 'A_seg_len': _A_in['seg_len'], 'B_x': _B_in['x'], 'B_w_gate': _B_in['w_gate'], 'B_w_up': _B_in['w_up'], 'B_w_down': _B_in['w_down'], 'c': _w((lambda *s: torch.randn(*s, dtype=torch.bfloat16) * 0.2)(1, 512) * 8.0), 'w_mod': _w((lambda *s: torch.randn(*s, dtype=torch.bfloat16) * 0.2)(512, 6 * 512) * 512 ** (-0.5) * 8.0)}

def reference(A_x, A_mem0, A_Wq, A_Wk, A_Wv, A_Wo, A_seg_len, B_x, B_w_gate, B_w_up, B_w_down, c, w_mod):

    def _A_ref(x, mem0, Wq, Wk, Wv, Wo, seg_len):
        B, S, Dm = x.shape
        H, Dh = (Wq.shape[1], Wq.shape[2])
        M = mem0.shape[1]
        scale = Dh ** (-0.5)
        n_seg = S // seg_len
        L = M + seg_len
        idx = torch.arange(L)
        mask = (idx[:, None] < M) | (idx[:, None] >= idx[None, :])
        mem = mem0
        outs = []
        for s in range(n_seg):
            xs = x[:, s * seg_len:(s + 1) * seg_len, :].clone()
            h = torch.concatenate([mem, xs], axis=1)
            q = torch.einsum('btd,dhe->bhte', h, Wq)
            k = torch.einsum('btd,dhe->bhte', h, Wk)
            v = torch.einsum('btd,dhe->bhte', h, Wv)
            scores = torch.einsum('bhqe,bhke->bhqk', q, k) * scale
            scores = torch.where(mask[None, None], scores, -1000000000.0)
            p = _softmax(scores, axis=-1)
            o = torch.einsum('bhqk,bhke->bhqe', p, v)
            o = torch.einsum('bhte,hed->btd', o, Wo)
            mem = o[:, :M, :].clone()
            outs.append(o[:, M:, :])
        return torch.concatenate(outs, axis=1)

    def _B_ref(x, w_gate, w_up, w_down):
        g = torch.einsum('bsd,df->bsf', x, w_gate)
        u = torch.einsum('bsd,df->bsf', x, w_up)
        h = _gelu(g) * u
        return torch.einsum('bsf,fd->bsd', h, w_down)
    x = A_x
    mod = (_silu(c) @ w_mod).reshape(c.shape[0], 6, 1, -1)
    sa, ca, ga, sb, cb, gb = (mod[:, 0].clone(), mod[:, 1].clone(), mod[:, 2].clone(), mod[:, 3].clone(), mod[:, 4].clone(), mod[:, 5].clone())
    h = _layernorm(x, axis=-1) * (1 + ca) + sa
    x = x + ga * _A_ref(h, A_mem0, A_Wq, A_Wk, A_Wv, A_Wo, A_seg_len)
    h = _layernorm(x, axis=-1) * (1 + cb) + sb
    x = x + gb * _B_ref(h, B_w_gate, B_w_up, B_w_down)
    return x
```

```cpp
#include <hip/hip_runtime.h>
#include <stdint.h>

namespace mk {
typedef unsigned short u16;
typedef unsigned int u32;
typedef unsigned long long u64;
typedef __attribute__((ext_vector_type(2))) float f32x2;
typedef __attribute__((ext_vector_type(4))) float f32x4;
typedef __attribute__((ext_vector_type(2))) u32 u32x2;
typedef __attribute__((ext_vector_type(4))) u32 u32x4;
typedef __attribute__((ext_vector_type(8))) u32 u32x8;
typedef __attribute__((ext_vector_type(4))) short s16x4;
typedef __attribute__((ext_vector_type(2))) __bf16 bf16x2;
#define LDS3 __attribute__((address_space(3)))
#define DI __device__ __forceinline__

constexpr int NSEG = 64, NH = 32, DM = 512, HD = 4096, DF = 1376;

constexpr size_t WS_NONCE = 0;
constexpr size_t WS_FLAG  = 0x6800000;
constexpr int F_READY = 0;
constexpr int F_MOD   = 256;
constexpr int F_H     = 512;
constexpr int F_KV    = F_H  + 64 * 8;
constexpr int F_KM    = F_KV + 64 * 32;
constexpr int F_O     = F_KM + 64 * 128;
constexpr int F_OP    = F_O  + 64 * 32;
constexpr int F_H2    = F_OP + 64 * 16;
constexpr int F_ACT   = F_H2 + 64 * 8;
constexpr int F_END   = F_ACT + 64 * 32;
constexpr size_t WS_MOD  = 0x40000;
constexpr size_t WS_HOP  = 0x6000000;
constexpr size_t WS_H    = 0x200000;
constexpr size_t WS_X1   = 0x400000;
constexpr size_t WS_H2   = 0x600000;
constexpr size_t WS_ACT  = 0x800000;
constexpr size_t WS_KM   = 0xE00000;
constexpr size_t WS_VM   = 0x1200000;
constexpr size_t WS_OP   = 0x1A00000;
constexpr size_t WS_KX   = 0x2200000;
constexpr size_t WS_VX   = 0x3200000;
constexpr size_t WS_O    = 0x4200000;
constexpr size_t WS_END  = 0x5200000;
static_assert(WS_FLAG + (size_t)F_END * 128 <= 0x7000000, "flags overflow");

struct Params {
  const u16 *x, *mem0, *Wq, *Wk, *Wv, *Wo, *wg, *wu, *wd, *c, *wmod;
  u16* out;
  char* ws;
};

#define OT0()
#define OT(i)
#define OTW(slot)
#define TLM(b_, i_)
#define TLS(k_, s_)
DI float b2f(u32 v) { return __uint_as_float(v << 16); }
DI u32 pk2(float lo, float hi) {
  f32x2 v = {lo, hi};
  return __builtin_bit_cast(u32, __builtin_convertvector(v, bf16x2));
}
DI float rbf(float f) { return b2f(pk2(f, f) & 0xFFFFu); }
DI u32 nosent(u32 v) { return v < 0xFFFFFFFEu ? v : 0xFFFFFFFEu; }
DI float fadd(float a, float b) { float r; asm("v_add_f32 %0, %1, %2" : "=v"(r) : "v"(a), "v"(b)); return r; }
DI float fmul(float a, float b) { float r; asm("v_mul_f32 %0, %1, %2" : "=v"(r) : "v"(a), "v"(b)); return r; }
DI float fmax2(float a, float b) { float r; asm("v_max_f32 %0, %1, %2" : "=v"(r) : "v"(a), "v"(b)); return r; }
DI float fmax3(float a, float b, float c) { float r; asm("v_max3_f32 %0, %1, %2, %3" : "=v"(r) : "v"(a), "v"(b), "v"(c)); return r; }
#define PIN1(a) asm volatile("" : "+v"(a))
#define PIN2(a, b) asm volatile("" : "+v"(a), "+v"(b))
#define PIN4(a, b, c, d) asm volatile("" : "+v"(a), "+v"(b), "+v"(c), "+v"(d))
DI int rfl(int v) { return __builtin_amdgcn_readfirstlane(v); }
DI void bar() {
  asm volatile("" ::: "memory");
  __builtin_amdgcn_s_waitcnt(0xC07F);
  asm volatile("s_barrier" ::: "memory");
}
DI void wait_vm0() { asm volatile("s_waitcnt vmcnt(0)" ::: "memory"); }
DI void cbar() { asm volatile("" ::: "memory"); }
DI u32 lds_addr(const void LDS3* p) { return (u32)(size_t)p; }
DI s16x4 trd(const char LDS3* p) { return __builtin_amdgcn_ds_read_tr16_b64_v4i16((s16x4 LDS3*)p); }
DI u32x2 trd2(const char LDS3* p) { return __builtin_bit_cast(u32x2, trd(p)); }
DI u32x4 cat4(u32x2 a, u32x2 b) { return __builtin_shufflevector(a, b, 0, 1, 2, 3); }
DI u32x8 cat8(u32x4 a, u32x4 b) { return __builtin_shufflevector(a, b, 0, 1, 2, 3, 4, 5, 6, 7); }

#define GLDS1(ldsb, voff, sbase, imm, SC) do { u32 keep_; \
  asm volatile("s_mov_b32 %0, m0\n\ts_mov_b32 m0, %1\n\ts_nop 0\n\tglobal_load_lds_dwordx4 %2, %3 offset:%4" SC "\n\ts_mov_b32 m0, %0" \
    : "=&s"(keep_) : "s"(ldsb), "v"(voff), "s"(sbase), "i"(imm) : "memory"); } while (0)
#define GLDS2(ldsb, voff, sbase, SC) do { u32 keep_; \
  asm volatile("s_mov_b32 %0, m0\n\ts_mov_b32 m0, %1\n\ts_nop 0\n\tglobal_load_lds_dwordx4 %2, %3" SC "\n\tglobal_load_lds_dwordx4 %2, %3 offset:1024" SC "\n\ts_mov_b32 m0, %0" \
    : "=&s"(keep_) : "s"(ldsb), "v"(voff), "s"(sbase) : "memory"); } while (0)
#define GLDS4(ldsb, voff, sbase, SC) do { u32 keep_; \
  asm volatile("s_mov_b32 %0, m0\n\ts_mov_b32 m0, %1\n\ts_nop 0\n\tglobal_load_lds_dwordx4 %2, %3" SC "\n\tglobal_load_lds_dwordx4 %2, %3 offset:1024" SC \
    "\n\tglobal_load_lds_dwordx4 %2, %3 offset:2048" SC "\n\tglobal_load_lds_dwordx4 %2, %3 offset:3072" SC "\n\ts_mov_b32 m0, %0" \
    : "=&s"(keep_) : "s"(ldsb), "v"(voff), "s"(sbase) : "memory"); } while (0)
#define GLDSV(ldsb, vaddr, SC) do { u32 keep_; \
  asm volatile("s_mov_b32 %0, m0\n\ts_mov_b32 m0, %1\n\ts_nop 0\n\tglobal_load_lds_dwordx4 %2, off" SC "\n\ts_mov_b32 m0, %0" \
    : "=&s"(keep_) : "s"(ldsb), "v"(vaddr) : "memory"); } while (0)
#define SC1 " sc1"
#define NOSC ""

typedef __amdgpu_buffer_rsrc_t rsrc_t;
DI rsrc_t mkrsrc(const void* p) { return __builtin_amdgcn_make_buffer_rsrc((void*)p, 0, 0x7FFFFFFF, 0x00020000); }
DI void st8(rsrc_t r, u32 voff, u32 soff, u32x2 v)  { __builtin_amdgcn_raw_buffer_store_b64(v, r, voff, soff, 16); }
DI void st16(rsrc_t r, u32 voff, u32 soff, u32x4 v) { __builtin_amdgcn_raw_buffer_store_b128(v, r, voff, soff, 16); }
DI u32x2 ld8(rsrc_t r, u32 voff, u32 soff)  { return __builtin_amdgcn_raw_buffer_load_b64(r, voff, soff, 16); }
DI u32x4 ld16(rsrc_t r, u32 voff, u32 soff) { return __builtin_amdgcn_raw_buffer_load_b128(r, voff, soff, 16); }
DI void flag_set(char* ws, int idx, u64 tag, int l) {
  if (l < 8) {
    u64 a, b;
    asm volatile("v_mov_b64 %0, %2\n\tv_mov_b64 %1, %2" : "=v"(a), "=v"(b) : "s"(tag));
    st16(mkrsrc(ws), (u32)WS_FLAG + (u32)idx * 128 + l * 16, 0, u32x4{(u32)a, (u32)(a >> 32), (u32)b, (u32)(b >> 32)});
  }
}
DI u64 flag_get(const char* ws, int idx) {
  return __hip_atomic_load((const u64*)(ws + WS_FLAG + (size_t)idx * 128), __ATOMIC_RELAXED, __HIP_MEMORY_SCOPE_AGENT);
}
constexpr int SPIN_CAP = 1 << 18;
DI void wait_flags(const char* ws, int idx0, int n, u64 tag, int lane, int& cap) {
  int it = 0;
#pragma unroll 1
  for (; it < cap; ++it) {
    cbar();
    bool ok = true;
    if (lane < n) ok = flag_get(ws, idx0 + lane) == tag;
    if (__all(ok)) break;
    __builtin_amdgcn_s_sleep(1);
  }
  if (it >= cap) cap = 1;
  cbar();
}

DI void wait_flags_idx(const char* ws, int idx, u64 tag, int& cap) {
  int it = 0;
#pragma unroll 1
  for (; it < cap; ++it) {
    cbar();
    bool ok = true;
    if (idx >= 0) ok = flag_get(ws, idx) == tag;
    if (__all(ok)) break;
    __builtin_amdgcn_s_sleep(1);
  }
  if (it >= cap) cap = 1;
  cbar();
}
DI int wait_votes(const char* ws, int idx0, u64 tag, int lane, int& cap) {
  int it = 0;
  u64 f = 0;
#pragma unroll 1
  for (; it < cap; ++it) {
    cbar();
    f = flag_get(ws, idx0 + lane) ^ tag;
    if (__all((f >> 1) == 0)) break;
    __builtin_amdgcn_s_sleep(1);
  }
  if (it >= cap) cap = 1;
  cbar();
  return __builtin_popcountll(__builtin_amdgcn_ballot_w64((f & 1) != 0));
}
#define WAIT_VM(n) asm volatile("s_waitcnt vmcnt(" #n ")" ::: "memory")

#define SMF_VV(acc, a, b, idx) asm volatile("v_smfmac_f32_16x16x64_bf16 %0, %1, %2, %3" : "+v"(acc) : "v"(a), "v"(b), "v"(idx))
#define SMF_VA(acc, a, b, idx) asm volatile("v_smfmac_f32_16x16x64_bf16 %0, %1, %2, %3" : "+v"(acc) : "v"(a), "a"(b), "v"(idx))
#define MF32_0(acc, a, b)  asm volatile("v_mfma_f32_16x16x32_bf16 %0, %1, %2, 0"  : "=&v"(acc) : "v"(a), "v"(b))
#define MF32(acc, a, b)    asm volatile("v_mfma_f32_16x16x32_bf16 %0, %1, %2, %0" : "+v"(acc) : "v"(a), "v"(b))
#define MF16(acc, a, b)    asm volatile("v_mfma_f32_16x16x16_bf16 %0, %1, %2, %0" : "+v"(acc) : "v"(a), "v"(b))
#define MF16_0(acc, a, b)  asm volatile("v_mfma_f32_16x16x16_bf16 %0, %1, %2, 0"  : "=&v"(acc) : "v"(a), "v"(b))
#define MF32_A0(acc, a, b) asm volatile("v_mfma_f32_16x16x32_bf16 %0, %1, %2, 0"  : "=&v"(acc) : "a"(a), "v"(b))
#define MF32_A(acc, a, b)  asm volatile("v_mfma_f32_16x16x32_bf16 %0, %1, %2, %0" : "+v"(acc) : "a"(a), "v"(b))
#define NOP2() asm volatile("s_nop 1")
#define MFWAIT1(a)          asm volatile("s_nop 7" : "+v"(a))
#define MFWAIT2(a, b)       asm volatile("s_nop 7" : "+v"(a), "+v"(b))
#define MFWAIT3(a, b, c)    asm volatile("s_nop 7" : "+v"(a), "+v"(b), "+v"(c))
#define MFWAIT4(a, b, c, d) asm volatile("s_nop 7" : "+v"(a), "+v"(b), "+v"(c), "+v"(d))

DI void swap32(u32& a, u32& b) { auto r = __builtin_amdgcn_permlane32_swap(a, b, false, false); a = r[0]; b = r[1]; }
DI void swap16(u32& a, u32& b) { auto r = __builtin_amdgcn_permlane16_swap(a, b, false, false); a = r[0]; b = r[1]; }
DI void swap32f(float& a, float& b) { u32 x = __float_as_uint(a), y = __float_as_uint(b); swap32(x, y); a = __uint_as_float(x); b = __uint_as_float(y); }
DI void swap16f(float& a, float& b) { u32 x = __float_as_uint(a), y = __float_as_uint(b); swap16(x, y); a = __uint_as_float(x); b = __uint_as_float(y); }
DI float gmax4(float m) { float a = m, b = m; swap16f(a, b); m = fmaxf(a, b); a = m; b = m; swap32f(a, b); return fmaxf(a, b); }
DI float gsum4(float m) { float a = m, b = m; swap16f(a, b); m = a + b; a = m; b = m; swap32f(a, b); return a + b; }

DI u32 img_sw(u32 row) { return ((row & 3) << 2) | ((row >> 2) & 3); }
DI u32 img_off(u32 row, u32 ch) { return 256u * row + 16u * (ch ^ img_sw(row)); }

DI int pm(int m, int t) { return 8 * (m >> 2) + (m & 3) + 4 * t; }
DI u32x2 pk4(f32x4 v) { return u32x2{pk2(v[0], v[1]), pk2(v[2], v[3])}; }
DI float blo(u32 v) { return __uint_as_float(v << 16); }
DI float bhi(u32 v) { return __uint_as_float(v & 0xFFFF0000u); }

DI u64 make_tag(u64 g) {
  u64 t = (g ^ (g >> 29)) * 0xBF58476D1CE4E5B9ull + 0x9E3779B97F4A7C15ull;
  t ^= t >> 32; t *= 0x94D049BB133111EBull; t ^= t >> 31;
  return ((t ^ g) >> 1) == 0 ? t ^ 2ull : t;
}
}
namespace mk {
constexpr int R_M = 0, R_KX = 8192, R_VX = R_KX + 16384, R_QE = R_VX + 16384, R_KE = R_QE + 2048, R_PS = R_KE + 2048, R_SW = R_PS + 128, R_VF = R_PS + 512, R_FL = R_PS + 640, R_VO = R_VX + 8192, R_V2 = R_PS + 1024, R_WV = 98304, R_WO = 131072;
#define T_ALPHA 162
#define T_BETA 60
#define T_Q 68
#define T_CA 3340
#define T_CB 1250
#define KV_DEFER 0
#define T_RUN 2
#define T_XMAX 40
#define T_BUMP 2
#define NCOPY 2
DI u32 copy_off(u32 c) { return c == 0 ? 0u : (1u << 20) + (c << 20); }
DI u32 copy_flip(u32 c) { return (copy_off(c) >> 21) & 1; }
DI u32 rt32() { return (u32)__builtin_amdgcn_s_memrealtime(); }
DI void wait_until(u32& t, int q, bool strict = false) {
  u32 now = rt32();
  if (strict) { while ((int)(now - t) > 0) t += q; }
  if ((int)(now - t) >= q) { do { t += q; } while ((int)(now - t) >= q); return; }
#pragma unroll 1
  for (int i = 0; i < 4096 && (int)(now - t) < 0; ++i) now = rt32();
}
DI u32 img_sw2(u32 row) { return ((row & 1) << 3) | (((row >> 3) & 1) << 2) | (((row >> 2) & 1) << 1) | ((row >> 1) & 1); }
DI u32 img_off2(u32 row, u32 ch) { return 256u * row + 16u * (ch ^ img_sw2(row)); }
#define SB() __builtin_amdgcn_sched_barrier(0)
#define SMF_ "v_smfmac_f32_16x16x64_bf16 "
typedef float f32x2_ __attribute__((ext_vector_type(2)));
DI f32x4 zero4() {
  f32x2_ lo, hi;
  asm volatile("v_mov_b64 %0, 0\n\tv_mov_b64 %1, 0" : "=v"(lo), "=v"(hi));
  return __builtin_shufflevector(lo, hi, 0, 1, 2, 3);
}
DI u32x4 halves4() {
  u32 a, b, c, d;
  asm volatile("v_mov_b32 %0, 0x3f003f00\n\tv_mov_b32 %1, 0x3f003f00\n\tv_mov_b32 %2, 0x3f003f00\n\tv_mov_b32 %3, 0x3f003f00" : "=v"(a), "=v"(b), "=v"(c), "=v"(d));
  return u32x4{a, b, c, d};
}
DI u32x4 ones4() {
  f32x2_ lo, hi;
  asm volatile("v_mov_b64 %0, -1\n\tv_mov_b64 %1, -1" : "=v"(lo), "=v"(hi));
  return __builtin_bit_cast(u32x4, __builtin_shufflevector(lo, hi, 0, 1, 2, 3));
}
template <int C> DI float dpp_ror8(float v) { return __builtin_bit_cast(float, __builtin_amdgcn_update_dpp(0, __builtin_bit_cast(int, v), 0x128, 0xF, 0xF, true)); }
DI u32 dpp_ror8u(u32 v) { return (u32)__builtin_amdgcn_update_dpp(0, (int)v, 0x128, 0xF, 0xF, true); }

#define PF(i)
#define PT(i)
#define PS()

#define TP(i)
DI u32 opq(u32 v) { asm volatile("" : "+v"(v)); return v; }
DI void plain_v(const u32x4 (&af)[8], const char LDS3* sm, u32 vl16, int idx, f32x4& v0, f32x4& v1) {
  v0 = zero4(); v1 = zero4();
  asm volatile("s_nop 1" : "+v"(v0), "+v"(v1));
#pragma unroll
  for (int hf = 0; hf < 2; ++hf) {
    u32x8 b[2][4];
#pragma unroll
    for (int k = 0; k < 4; ++k)
#pragma unroll
      for (int t = 0; t < 2; ++t) {
        const char LDS3* p = sm + R_WV + (t * 8 + 4 * hf + k) * 2048 + vl16;
        b[t][k] = cat8(*(const u32x4 LDS3*)p, *(const u32x4 LDS3*)(p + 1024));
      }
    SB();
#pragma unroll
    for (int k = 0; k < 4; ++k) { SMF_VV(v0, af[4 * hf + k], b[0][k], idx); SMF_VV(v1, af[4 * hf + k], b[1][k], idx); }
    SB();
  }
  MFWAIT2(v0, v1);
}

DI void mem_kv_flag(char* ws, u32 lane, int sh, int j, u64 tag) {
  wait_vm0();
  flag_set(ws, F_KM + sh * 4 + j, tag, opq(lane));
}
DI void mem_kv(const u32x4 (&af)[8], const char LDS3* sm, rsrc_t rws, u32 lane_, u32 qe_r, int idx, int sh, int j) {
  const u32 lane = opq(lane_), n = lane & 15, g = lane >> 4;
  const u32x4 kv = cat4(trd2(sm + R_KE + qe_r + j * 512), trd2(sm + R_KE + qe_r + j * 512 + 64));
  if (n < 8) st16(rws, WS_KM + (sh * 4 + j) * 512 + (g * 8 + n) * 16, 0, kv);
  f32x4 v0, v1;
  plain_v(af, sm, lane * 16, idx, v0, v1);
  st8(rws, WS_VM + (sh * 8 + 2 * j) * 512 + lane * 8, 0, pk4(v0));
  st8(rws, WS_VM + (sh * 8 + 2 * j + 1) * 512 + lane * 8, 0, pk4(v1));
}
DI void vx_wo(char LDS3* sm, u32 lane_, int u0, int u1, u32 dst) {
  const u32 lane = opq(lane_), vl16 = lane * 16;
  const u32 vt_r = ((lane & 3) * 16 + 4 * (lane >> 4) + ((lane >> 2) & 3)) * 16;
  u32x4 va[2][4];
#pragma unroll
  for (int k = 0; k < 4; ++k)
#pragma unroll
    for (int t = 0; t < 2; ++t) {
      const char LDS3* vp = sm + R_VX + 2 * k * 1024 + vt_r + 8 * t;
      va[t][k] = cat4(trd2(vp), trd2(vp + 1024));
    }
#pragma unroll 1
  for (int u = u0; u < u1; ++u) {
    u32x4 wo[2][4];
#pragma unroll
    for (int k = 0; k < 4; ++k)
#pragma unroll
      for (int c = 0; c < 2; ++c) wo[c][k] = *(const u32x4 LDS3*)(sm + R_WO + u * 8192 + (c * 4 + k) * 1024 + vl16);
    SB();
    f32x4 dv[2][2];
#pragma unroll
    for (int k = 0; k < 4; ++k)
#pragma unroll
      for (int c = 0; c < 2; ++c)
#pragma unroll
        for (int t = 0; t < 2; ++t) {
          if (k == 0) MF32_0(dv[c][t], va[t][0], wo[c][0]); else MF32(dv[c][t], va[t][k], wo[c][k]);
        }
    MFWAIT4(dv[0][0], dv[0][1], dv[1][0], dv[1][1]);
#pragma unroll
    for (int c = 0; c < 2; ++c) *(u32x4 LDS3*)(sm + dst + u * 2048 + c * 1024 + vl16) = cat4(pk4(dv[c][0]), pk4(dv[c][1]));
    SB();
  }
}
DI bool any_ones(const u32x4 (&af)[8]) {
  u32 m = 0;
#pragma unroll
  for (int k = 0; k < 8; ++k) { m = max(max(m, af[k][0]), af[k][1]); m = max(max(m, af[k][2]), af[k][3]); }
  return m == ~0u;
}

DI void rec_role(const Params& P, int ri, u64 tag, char LDS3* sm) {
  const int tid = threadIdx.x, lane = tid & 63, w = rfl(tid >> 6);
  const int h = ri >> 2, j = ri & 3;
  const int n = lane & 15, g = lane >> 4, q4 = n >> 2, p4 = n & 3;
  char* ws = P.ws;
  const u32 smb = lds_addr(sm);
  int cap = SPIN_CAP;

  u32x8 wq[2][8], wk[2][8], wv[2][8];
  {
    const u32 voff = g * 8192 + ((n ^ (((g & 1) << 3) | ((w >> 1) << 2) | ((w & 1) << 1) | (g >> 1))) << 4);
    const char* b0 = (const char*)P.Wq + h * 256;
    const char* b1 = (const char*)P.Wk + h * 256;
    const char* b2 = (const char*)P.Wv + h * 256;
    auto issue = [&](int s, int buf) {
#pragma unroll
      for (int m = 0; m < 4; ++m) {
        const int i = w + 4 * m;
        const size_t go = (size_t)(64 * s + 4 * i) * 8192;
        const u32 lo = smb + buf * 49152 + i * 1024;
        GLDS1(lo, voff, b0 + go, 0, NOSC);
        GLDS1(lo + 16384, voff, b1 + go, 0, NOSC);
        GLDS1(lo + 32768, voff, b2 + go, 0, NOSC);
      }
    };
    {
      const u32 voffo = g * 1024 + ((n ^ ((g << 2) | w)) << 4);
#pragma unroll
      for (int m = 0; m < 8; ++m) {
        const int i = w + 4 * m;
        GLDS1(smb + R_WO + i * 1024, voffo, (const char*)P.Wo + (size_t)(h * 128 + 4 * i) * 1024 + j * 256, 0, NOSC);
      }
    }
    issue(0, 0); issue(1, 1);
    u32x4 wo[2][4];
    WAIT_VM(24);
    bar();
#pragma unroll
    for (int t = 0; t < 2; ++t)
#pragma unroll
      for (int ks = 0; ks < 4; ++ks) {
        const u32 o0 = img_off(32 * ks + 8 * g + q4, 4 * w + p4) + 8 * t, o1 = img_off(32 * ks + 8 * g + 4 + q4, 4 * w + p4) + 8 * t;
        wo[t][ks] = cat4(trd2(sm + R_WO + o0), trd2(sm + R_WO + o1));
      }
    bar();
#pragma unroll
    for (int t = 0; t < 2; ++t)
#pragma unroll
      for (int ks = 0; ks < 4; ++ks) *(u32x4 LDS3*)(sm + R_WO + ((w * 2 + t) * 4 + ks) * 1024 + lane * 16) = wo[t][ks];
#pragma unroll
    for (int s = 0; s < 8; ++s) {
      if (s < 7) WAIT_VM(12);
      else WAIT_VM(0);
      bar();
      const char LDS3* bp = sm + (s & 1) * 49152;
      u32x4 fu[2][2];
#pragma unroll
      for (int ks = 0; ks < 2; ++ks) {
        const u32 dr = 16 * (2 * ks + (n >> 3)) + 4 * ((n >> 2) & 1) + 8 * ((n >> 1) & 1) + (n & 1);
        f32x4 du[2][2];
#pragma unroll
        for (int a = 0; a < 2; ++a) {
          u32x4 va[4];
#pragma unroll
          for (int k = 0; k < 4; ++k) va[k] = *(const u32x4 LDS3*)(bp + 32768 + img_off2(dr + 2 * a, 4 * k + g));
#pragma unroll
          for (int k = 0; k < 4; ++k) {
            if (k == 0) { MF32_0(du[0][a], va[0], wo[0][0]); MF32_0(du[1][a], va[0], wo[1][0]); }
            else { MF32(du[0][a], va[k], wo[0][k]); MF32(du[1][a], va[k], wo[1][k]); }
          }
        }
        MFWAIT4(du[0][0], du[0][1], du[1][0], du[1][1]);
        fu[0][ks] = cat4(pk4(du[0][0]), pk4(du[0][1])); fu[1][ks] = cat4(pk4(du[1][0]), pk4(du[1][1]));
      }
#pragma unroll
      for (int t = 0; t < 2; ++t) {
        u32x4 f[2][2];
#pragma unroll
        for (int ks = 0; ks < 2; ++ks) {
          const u32 r0 = 16 * (2 * ks + (g >> 1)) + 8 * (q4 >> 1) + 4 * (g & 1) + (q4 & 1), r1 = r0 + 2;
          const u32 o0 = img_off2(r0, 2 * (2 * w + t) + (p4 >> 1)) + 8 * (p4 & 1), o1 = img_off2(r1, 2 * (2 * w + t) + (p4 >> 1)) + 8 * (p4 & 1);
#pragma unroll
          for (int m = 0; m < 2; ++m) f[m][ks] = cat4(trd2(bp + m * 16384 + o0), trd2(bp + m * 16384 + o1));
          if (w == j) {
            const u32 v0 = img_off2(r0, 4 * w + p4) + 8 * t, v1 = img_off2(r1, 4 * w + p4) + 8 * t;
            *(u32x4 LDS3*)(sm + R_WV + ((t * 8 + s) * 2 + ks) * 1024 + lane * 16) = cat4(trd2(bp + 32768 + v0), trd2(bp + 32768 + v1));
          }
        }
        wq[t][s] = cat8(f[0][0], f[0][1]); wk[t][s] = cat8(f[1][0], f[1][1]); wv[t][s] = cat8(fu[t][0], fu[t][1]);
        asm volatile("" : "+a"(wq[t][s]), "+a"(wk[t][s]), "+v"(wv[t][s]));
      }
      bar();
      if (s + 2 < 8) issue(s + 2, s & 1);
    }
  }

  TLM(blockIdx.x, 2);
  const rsrc_t rws = mkrsrc(ws);
  const u32 vl16 = lane * 16;
  u32 par;
  const u32 hop = (u32)(WS_HOP + ((0 - ((size_t)ws + WS_HOP)) & 0x1FFFFF));
  {
    const u32x4 ones = {~0u, ~0u, ~0u, ~0u};
    if (w < 2) {
#pragma unroll
      for (int m = 0; m < 4; ++m) st16(rws, hop + (64 + w * 32 + (ri >> 2)) * 16384 + (m >> 1) * 8192 + (ri & 3) * 2048 + (m & 1) * 1024 + vl16, 0, ones);
    } else if (w == 2) {
#pragma unroll
      for (int c = 0; c < NCOPY; ++c)
#pragma unroll
        for (int m = 0; m < 8; ++m)
          if ((m & 3) != 0 || lane >= 4) st16(rws, hop + copy_off(c) + (16 * (m & 3) + (lane >> 2)) * 16384 + (m >> 2) * 8192 + ri * 64 + (lane & 3) * 16, 0, ones);
    }
    if (w == 2 && ri < 64) st16(rws, hop + (3u << 20) + ri * 16384 + (lane >> 5) * 8192 + (lane & 31) * 16, 0, ones);
    if (w == 3 && ri < 64) {
      const u32 a0 = hop + ri * 128 + (ri & 1) * 8192, a1 = a0 ^ 8192;
      if (lane == 0) { st16(rws, a0, 0, ones); st16(rws, a1, 0, ones); }
      wait_vm0();
      const u32 t0 = (u32)__builtin_amdgcn_s_memtime();
      u32x4 v0 = ld16(rws, a0, 0);
      asm volatile("s_waitcnt vmcnt(0)" : "+v"(v0) :: "memory");
      const u32 t1 = (u32)__builtin_amdgcn_s_memtime();
      u32x4 v1 = ld16(rws, a1, 0);
      asm volatile("s_waitcnt vmcnt(0)" : "+v"(v1) :: "memory");
      const u32 t2 = (u32)__builtin_amdgcn_s_memtime();
      const u32 first_faster = (t1 - t0) < (t2 - t1);
      if (lane == 0) *(u32 LDS3*)(sm + R_PS) = first_faster ? (ri & 1) : (ri & 1) ^ 1;
    }
    for (int i = tid; i < 4096; i += 256) {
      const int t = i >> 9, d = i & 511;
      *(u16 LDS3*)(sm + R_M + ((d >> 3) * 8 + t) * 16 + (d & 7) * 2) = P.mem0[i];
    }
    wait_vm0(); bar();
    flag_set(ws, F_READY + ri, tag ^ (u64)(ri < 64 ? rfl(*(const u32 LDS3*)(sm + R_PS)) & 1 : 0), tid);
    if (w == 0) {
      const int votes = wait_votes(ws, F_READY, tag, lane, cap);
      wait_votes(ws, F_READY + 64, tag, lane, cap);
      if (lane == 0) *(u32 LDS3*)(sm + R_PS + 4) = votes > 32;
    }
    TLM(blockIdx.x, 3);
    if (w == 1) { wait_flags(ws, F_KV + h, 1, tag, lane, cap); wait_flags(ws, F_KV + 32 + h, 1, tag, lane, cap); }
    bar();
    par = rfl(*(const u32 LDS3*)(sm + R_PS + 4)) & 1;
    GLDS2(smb + R_KX + w * 2048, vl16, ws + WS_KX + (size_t)h * 8192 + w * 2048, SC1);
    GLDS2(smb + R_VX + w * 2048, vl16, ws + WS_VX + (size_t)h * 8192 + w * 2048, SC1);
    wait_vm0();
    bar();
    vx_wo(sm, lane, w, w + 1, R_VO);
  }

  constexpr int tA = T_ALPHA, tB = T_BETA, tQ = T_Q, tCA = T_CA, tCB = T_CB;
  const u32 hopm = hop + par * 8192;
  const u32 cown = (ri >> 5) * NCOPY >> 2;
  const u32 memr = hop + copy_off(cown) + (par ^ copy_flip(cown)) * 8192;
  const u32 sched = hop + (3u << 20) + ((par ^ 1) << 13);
  const u32 hopr = hopm + (64 + (ri >> 2)) * 16384 + (ri & 3) * 2048;
  TLM(blockIdx.x, 4);
  int capB = SPIN_CAP;
  const int idx = (n & 8) ? 0xEEEE : 0x4444;
  const int wred = (j + 1) & 3, wflag = (j + 2) & 3, wrst = (j + 3) & 3;
  const u32 qe_w = (32 * w + 16 * (g >> 1) + n) * 16 + (g & 1) * 8;
  const u32 qe_r = (8 * g + q4) * 16 + (p4 & 1) * 8;
  const u32 prid = 2 * (16 * j + 4 * w + g) + ((n >> 2) & 1);
  const u32 part_w = hopm + (64 + (prid >> 2)) * 16384 + (prid & 3) * 2048 + h * 64 + (n & 3) * 16;
  const float SC = 0.08838834764831845f * 1.4426950408889634f;
  const bool lo8 = n < 8;
  const u64 lo8m = 0x00FF00FF00FF00FFull;
  u32 T = 0;
  u32 sw = ((u32)tA << 8) | tB;
  u32 ra_ = rt32(), ca_ = (u32)__builtin_amdgcn_s_memtime(), rb_ = ra_, cb_ = ca_, extra = 0, bump = T_BUMP;
  int run = 0, hold = 0;
  u32 rlast = 0, len = tA + tB;
  if (tid < 8) *(u32 LDS3*)(sm + R_SW + 128 * (tid >> 2) + 16 * (tid & 3)) = sw;
  if (tid == 8) *(u32 LDS3*)(sm + R_VF) = 0;

#define LN() (opq(vl16) >> 4)
#pragma unroll 1
  for (int s = 0; s < NSEG - 1; ++s) {
    const int cb = s & 1;
    f32x4 aq[2], ak[2], av[2];
    u32x4 af[8], kb[4], kc[4];
    auto phase1 = [&]() __attribute__((always_inline)) -> bool {
      aq[0] = zero4(); aq[1] = zero4(); ak[0] = zero4(); ak[1] = zero4();
      if (w == wred) { wait_vm0(); PT(0); }
      bar();
      PS(); TP(0);
#pragma unroll
      for (int k = 0; k < 8; ++k) af[k] = *(const u32x4 LDS3*)(sm + R_M + k * 1024 + vl16);
      const char LDS3* kx = sm + R_KX + cb * 8192 + vl16;
#pragma unroll
      for (int k = 0; k < 4; ++k) kb[k] = *(const u32x4 LDS3*)(kx + k * 1024);
      SB();
      u32 mw_;
      asm volatile(SMF_ "%0, %3, %4, %6\n\tv_max_u32 %2, %7, %8\n\t" SMF_ "%1, %3, %5, %6\n\tv_max3_u32 %2, %2, %9, %10"
                   : "+v"(aq[0]), "+v"(aq[1]), "=&v"(mw_)
                   : "v"(af[0]), "a"(wq[0][0]), "a"(wq[1][0]), "v"(idx), "v"(af[0][0]), "v"(af[0][1]), "v"(af[0][2]), "v"(af[0][3]));
      SB();
#pragma unroll
      for (int k = 1; k < 8; ++k) {
        asm volatile(SMF_ "%0, %3, %4, %6\n\tv_max3_u32 %2, %2, %7, %8\n\t" SMF_ "%1, %3, %5, %6\n\tv_max3_u32 %2, %2, %9, %10"
                     : "+v"(aq[0]), "+v"(aq[1]), "+v"(mw_)
                     : "v"(af[k]), "a"(wq[0][k]), "a"(wq[1][k]), "v"(idx), "v"(af[k][0]), "v"(af[k][1]), "v"(af[k][2]), "v"(af[k][3]));
        SB();
      }
      TP(1);
      SMF_VA(ak[0], af[0], wk[0][0], idx);
      const bool miss = __any(mw_ == ~0u);
      SB();
      SMF_VA(ak[1], af[0], wk[1][0], idx);
      return miss;
    };
    auto gather = [&]() __attribute__((always_inline)) {
      if (w == wred && s > 0) {
        wait_until(T, tQ);
        PS();
        const char* src = ws + memr + (u32)s * 16384;
        GLDS4(smb + R_M, vl16, src, SC1);
        GLDS4(smb + R_M + 4096, vl16, src + 4096, SC1);
      }
    };
    gather();
    {
        asm volatile("s_barrier" ::: "memory");
        const bool pf = s + 1 < NSEG - 1;
        const size_t go = ((size_t)(s + 1) * 32 + h) * 8192;
        if (w == wflag) {
          if (pf) {
            GLDS4(smb + R_KX + (cb ^ 1) * 8192, vl16, ws + WS_KX + go, SC1);
            GLDS4(smb + R_KX + (cb ^ 1) * 8192 + 4096, vl16, ws + WS_KX + go + 4096, SC1);
          }
        } else if (w == wrst) {
          if (s > 0) {
            const u32x4 ones = ones4();
            const u32 ro = hopr + (cb ^ 1) * 524288 + opq(vl16);
            st16(rws, ro, 0, ones);
            st16(rws, ro + 1024, 0, ones);
          }
          if (pf) {
            GLDS4(smb + R_VX, vl16, ws + WS_VX + go, SC1);
            GLDS4(smb + R_VX + 4096, vl16, ws + WS_VX + go + 4096, SC1);
          } else wait_vm0();
        }
        if (KV_DEFER && w == j && s > 0) mem_kv_flag(ws, LN(), (s - 1) * 32 + h, j, tag);
        if (w == wflag) { if (pf) WAIT_VM(8); else wait_vm0(); }
    }
    if (__builtin_expect(phase1(), 0)) {
#pragma unroll 1
      for (int it = 0; ; ++it) {
        if (it >= capB) { capB = 0; break; }
        T += tQ;
        MFWAIT4(aq[0], aq[1], ak[0], ak[1]);
        bar();
        gather();
        if (!phase1()) break;
      }
    }
    SB();
    SMF_VA(ak[0], af[1], wk[0][1], idx);
    PIN2(aq[0], aq[1]);
    SB();
    {
      f32x2_ q01, q23;
      float a0 = aq[0][0], b0 = aq[1][0], a1 = aq[0][1], b1 = aq[1][1];
      swap32f(a0, b0); swap32f(a1, b1);
      q01 = f32x2_{a0, a1} + f32x2_{b0, b1};
      SB();
      SMF_VA(ak[1], af[1], wk[1][1], idx);
      SB();
      float a2 = aq[0][2], b2 = aq[1][2], a3 = aq[0][3], b3 = aq[1][3];
      swap32f(a2, b2); swap32f(a3, b3);
      q23 = f32x2_{a2, a3} + f32x2_{b2, b3};
      *(u32x2 LDS3*)(sm + R_QE + qe_w) = u32x2{pk2(q01[0], q01[1]), pk2(q23[0], q23[1])};
    }
    TP(2);
    SB();
    SMF_VA(ak[0], af[2], wk[0][2], idx); SMF_VA(ak[1], af[2], wk[1][2], idx);
    SMF_VA(ak[0], af[3], wk[0][3], idx); SMF_VA(ak[1], af[3], wk[1][3], idx);
    SB();
    if (ri == 0) TLS(0, s);
    bar();
    PF(1); TP(3);
    u32x4 qf[4];
#pragma unroll
    for (int k = 0; k < 4; ++k) qf[k] = cat4(trd2(sm + R_QE + qe_r + k * 512), trd2(sm + R_QE + qe_r + k * 512 + 64));
    const char LDS3* kx2 = sm + R_KX + cb * 8192 + 4096 + vl16;
    kc[0] = *(const u32x4 LDS3*)kx2; kc[1] = *(const u32x4 LDS3*)(kx2 + 1024);
    SB();
#pragma unroll
    for (int k = 4; k < 8; ++k) { SMF_VA(ak[0], af[k], wk[0][k], idx); SMF_VA(ak[1], af[k], wk[1][k], idx); }
    TP(4);
    SB();
    f32x4 sA, sB, sC;
    MF32_0(sB, kb[0], qf[0]); MF32(sB, kb[1], qf[1]);
    kc[2] = *(const u32x4 LDS3*)(kx2 + 2048); kc[3] = *(const u32x4 LDS3*)(kx2 + 3072);
    SB();
    MF32(sB, kb[2], qf[2]);
    PIN2(ak[0], ak[1]);
    SB();
    {
      float a0 = ak[0][0], b0 = ak[1][0], a1 = ak[0][1], b1 = ak[1][1], a2 = ak[0][2], b2 = ak[1][2], a3 = ak[0][3], b3 = ak[1][3];
      swap32f(a0, b0);
      SB(); MF32(sB, kb[3], qf[3]); SB();
      swap32f(a1, b1);
      SB(); MF32_0(sC, kc[0], qf[0]); SB();
      swap32f(a2, b2);
      SB(); MF32(sC, kc[1], qf[1]); SB();
      swap32f(a3, b3);
      const f32x2_ k01 = f32x2_{a0, a1} + f32x2_{b0, b1}, k23 = f32x2_{a2, a3} + f32x2_{b2, b3};
      *(u32x2 LDS3*)(sm + R_KE + qe_w) = u32x2{pk2(k01[0], k01[1]), pk2(k23[0], k23[1])};
      TP(5);
      SB();
      MF32(sC, kc[2], qf[2]);
      av[0] = zero4();
      SB();
      MF32(sC, kc[3], qf[3]);
      av[1] = zero4();
      SB();
    }
    u32 h0, h1, h2, h3;
#define DPP8 " row_ror:8 row_mask:0xf bank_mask:0xf\n\t"
    asm volatile(
        "v_mov_b32 %2, 0x3f003f00\n\t" SMF_ "%0, %6, %8, %12\n\t"
        "v_mov_b32 %3, 0x3f003f00\n\t" SMF_ "%1, %6, %9, %12\n\t"
        "v_mov_b32 %4, 0x3f003f00\n\t" SMF_ "%0, %7, %10, %12\n\t"
        "v_mov_b32 %5, 0x3f003f00\n\t" SMF_ "%1, %7, %11, %12"
        : "+v"(av[0]), "+v"(av[1]), "=&v"(h0), "=&v"(h1), "=&v"(h2), "=&v"(h3)
        : "v"(af[0]), "v"(af[1]), "v"(wv[0][0]), "v"(wv[1][0]), "v"(wv[0][1]), "v"(wv[1][1]), "v"(idx));
    const u32x4 hf = {h0, h1, h2, h3};
    WAIT_VM(8);
    bar();
    PF(2); TP(6);
    u32x4 kmf[4];
#pragma unroll
    for (int k = 0; k < 4; ++k) kmf[k] = cat4(trd2(sm + R_KE + qe_r + k * 512), trd2(sm + R_KE + qe_r + k * 512 + 64));
    SB();
    float sx0, sx1, sx2, sx3, m1, t1;
    asm volatile(
        SMF_ "%0, %8, %12, %19\n\tv_cndmask_b32 %2, %25, %21, %20\n\t"
        SMF_ "%1, %8, %13, %19\n\tv_cndmask_b32 %3, %26, %22, %20\n\t"
        SMF_ "%0, %9, %14, %19\n\tv_cndmask_b32 %4, %27, %23, %20\n\t"
        SMF_ "%1, %9, %15, %19\n\tv_cndmask_b32 %5, %28, %24, %20\n\t"
        SMF_ "%0, %10, %16, %19\n\tv_max3_f32 %6, %2, %3, %4\n\t"
        SMF_ "%1, %10, %17, %19\n\tv_max_f32 %6, %6, %5\n\t"
        SMF_ "%0, %11, %18, %19\n\ts_nop 0\n\tv_max_f32_dpp %6, %6, %6" DPP8 "v_mov_b32 %7, %6"
        : "+v"(av[0]), "+v"(av[1]), "=&v"(sx0), "=&v"(sx1), "=&v"(sx2), "=&v"(sx3), "=&v"(m1), "=&v"(t1)
        : "v"(af[2]), "v"(af[3]), "v"(af[4]), "v"(af[5]), "v"(wv[0][2]), "v"(wv[1][2]), "v"(wv[0][3]), "v"(wv[1][3]), "v"(wv[0][4]), "v"(wv[1][4]), "v"(wv[0][5]),
          "v"(idx), "s"(lo8m), "v"(sB[0]), "v"(sB[1]), "v"(sB[2]), "v"(sB[3]), "v"(sC[0]), "v"(sC[1]), "v"(sC[2]), "v"(sC[3]));
    SB();
    MF32_0(sA, kmf[0], qf[0]); MF32(sA, kmf[1], qf[1]);
    asm volatile("v_permlane16_swap_b32 %0, %1\n\tv_max_f32 %0, %0, %1\n\tv_mov_b32 %1, %0" : "+v"(m1), "+v"(t1));
    MF32(sA, kmf[2], qf[2]); MF32(sA, kmf[3], qf[3]);
    TP(7);
    SB();
    const u32x4 vx0 = *(const u32x4 LDS3*)(sm + (cb ? R_V2 : R_VO) + w * 2048 + vl16);
    const u32x4 vx1 = *(const u32x4 LDS3*)(sm + (cb ? R_V2 : R_VO) + w * 2048 + 1024 + vl16);
    SB();
    float mx, tm, pA0, pA1, pA2, pA3;
    const float a0 = sA[0], a1 = sA[1], a2 = sA[2], a3 = sA[3];
    asm volatile(
        SMF_ "%1, %10, %13, %18\n\tv_permlane32_swap_b32 %8, %9\n\t"
        SMF_ "%0, %11, %14, %18\n\tv_max_f32 %8, %8, %9\n\t"
        SMF_ "%1, %11, %15, %18\n\t"
        "v_max3_f32 %2, %19, %20, %21\n\tv_max_f32 %2, %2, %22\n\tv_mov_b32 %3, %2\n\t"
        SMF_ "%0, %12, %16, %18\n\t"
        "s_nop 0\n\tv_permlane16_swap_b32 %2, %3\n\tv_max3_f32 %2, %2, %3, %8\n\tv_mul_f32 %3, 0xbe0293ee, %2\n\t"
        "v_fmamk_f32 %4, %19, 0x3e0293ee, %3\n\t"
        SMF_ "%1, %12, %17, %18\n\t"
        "v_fmamk_f32 %5, %20, 0x3e0293ee, %3\n\tv_fmamk_f32 %6, %21, 0x3e0293ee, %3\n\tv_fmamk_f32 %7, %22, 0x3e0293ee, %3"
        : "+v"(av[0]), "+v"(av[1]), "=&v"(mx), "=&v"(tm), "=&v"(pA0), "=&v"(pA1), "=&v"(pA2), "=&v"(pA3), "+v"(m1), "+v"(t1)
        : "v"(af[5]), "v"(af[6]), "v"(af[7]), "v"(wv[1][5]), "v"(wv[0][6]), "v"(wv[1][6]), "v"(wv[0][7]), "v"(wv[1][7]), "v"(idx), "v"(a0), "v"(a1), "v"(a2), "v"(a3));
    u32 x0, x1, x2, x3, pa0, pa1;
    asm volatile(
        "v_fmamk_f32 %6, %6, 0x3e0293ee, %14\n\tv_fmamk_f32 %7, %7, 0x3e0293ee, %14\n\tv_fmamk_f32 %8, %8, 0x3e0293ee, %14\n\tv_fmamk_f32 %9, %9, 0x3e0293ee, %14\n\t"
        "v_exp_f32 %6, %6\n\tv_exp_f32 %7, %7\n\tv_exp_f32 %8, %8\n\tv_exp_f32 %9, %9\n\t"
        "v_exp_f32 %10, %10\n\tv_cvt_pk_bf16_f32 %0, %6, %7\n\tv_exp_f32 %11, %11\n\tv_cvt_pk_bf16_f32 %1, %8, %9\n\t"
        "v_exp_f32 %12, %12\n\tv_exp_f32 %13, %13\n\t"
        "v_mov_b32_dpp %2, %0" DPP8 "v_mov_b32_dpp %3, %1" DPP8
        "v_cvt_pk_bf16_f32 %4, %10, %11\n\tv_cvt_pk_bf16_f32 %5, %12, %13"
        : "=&v"(x0), "=&v"(x1), "=&v"(x2), "=&v"(x3), "=&v"(pa0), "=&v"(pa1), "+v"(sx0), "+v"(sx1), "+v"(sx2), "+v"(sx3), "+v"(pA0), "+v"(pA1), "+v"(pA2), "+v"(pA3)
        : "v"(tm));
    SB();
    TP(8);
    u32x4 px = {x0, x1, x2, x3};
    u32x2 pa = {pa0, pa1};
    const u32x2 hf2 = __builtin_shufflevector(hf, hf, 0, 1);
    f32x4 o0, o1, sux, sum_;
    PIN2(av[0], av[1]);
    asm volatile("" : "+v"(px), "+v"(pa));
    MF32_0(sux, hf, px);
    u32x2 vmf0 = pk4(av[0]);
    asm volatile("" : "+v"(vmf0));
    SB();
    MF32_0(o0, vx0, px);
    u32x2 vmf1 = pk4(av[1]);
    asm volatile("" : "+v"(vmf1));
    SB();
    MF32_0(o1, vx1, px);
    MF16_0(sum_, hf2, pa);
    MF16(o0, vmf0, pa); MF16(o1, vmf1, pa);
    TP(9);
    f32x2_ u0, u1, u2, u3;
    {
      f32x2_ rr;
      float tot;
      asm volatile("s_nop 5\n\tv_fma_f32 %0, 2.0, %1, %2\n\tv_rcp_f32 %0, %0" : "=&v"(tot) : "v"(sux[0]), "v"(sum_[0]), "v"(sux), "v"(sum_));
      rr[0] = tot;
      const f32x2_ p0 = __builtin_shufflevector(o0, o0, 0, 1), p1 = __builtin_shufflevector(o0, o0, 2, 3), p2 = __builtin_shufflevector(o1, o1, 0, 1), p3 = __builtin_shufflevector(o1, o1, 2, 3);
      asm volatile("s_nop 0\n\tv_pk_mul_f32 %0, %5, %4 op_sel_hi:[1,0]\n\tv_pk_mul_f32 %1, %6, %4 op_sel_hi:[1,0]\n\tv_pk_mul_f32 %2, %7, %4 op_sel_hi:[1,0]\n\tv_pk_mul_f32 %3, %8, %4 op_sel_hi:[1,0]"
                   : "=&v"(u0), "=&v"(u1), "=&v"(u2), "=&v"(u3) : "v"(rr), "v"(p0), "v"(p1), "v"(p2), "v"(p3));
    }
    PF(3); TP(10);
    if (lo8) st16(rws, part_w, cb * 524288, u32x4{nosent(pk2(u0[0], u0[1])), nosent(pk2(u1[0], u1[1])), nosent(pk2(u2[0], u2[1])), nosent(pk2(u3[0], u3[1]))});
    PT(5);
    if (w == wred) {
      {
        const u32 sv = *(const u32 LDS3*)(sm + R_SW + cb * 128 + (LN() & 3) * 16);
        const u32 sm_ = max(max((u32)__builtin_amdgcn_readlane(sv, 0), (u32)__builtin_amdgcn_readlane(sv, 1)), max((u32)__builtin_amdgcn_readlane(sv, 2), (u32)__builtin_amdgcn_readlane(sv, 3)));
        if (sm_ != ~0u) sw = sm_;
      }
      if (s == 0) { T = rt32() + tQ; T -= T % (u32)tQ; } else T += sw >> 8;
      u32 m7 = n & 7;
      asm volatile("" : "+v"(m7));
      u32x4 sel;
#pragma unroll
      for (int k = 0; k < 4; ++k) sel[k] = (m7 == 2 * k ? 0x3F80u : 0u) | (m7 == 2 * k + 1 ? 0x3F800000u : 0u);
      const u32 ln = LN(), cc = (ln >> 2) & 3;
      u32 mw = hop + (cc == 0 ? 0u : (1u << 20) + (cc << 20)) + ((par ^ (cc == 1 || cc == 2)) << 13) + ri * 64 + (ln & 3) * 16 + (ln >> 4) * 8;
      asm volatile("" : "+v"(mw));
      f32x4 d;
      u32x4 y0, y1;
      auto reduce = [&]() __attribute__((always_inline)) -> bool {
        wait_until(T, tQ);
        PS();
        y0 = ld16(rws, vl16, hopr + cb * 524288); y1 = ld16(rws, vl16, hopr + cb * 524288 + 1024);
        asm volatile("s_waitcnt vmcnt(0)\n\tv_mfma_f32_16x16x32_bf16 %0, %3, %1, 0\n\tv_mfma_f32_16x16x32_bf16 %0, %3, %2, %0\n\ts_nop 7"
                     : "=&v"(d) : "v"(y0), "v"(y1), "v"(sel));
        float e0, e1, e2, e3, d0 = d[0], d1 = d[1], d2 = d[2], d3 = d[3];
        asm volatile("v_add_f32_dpp %0, %4, %4 row_ror:4 row_mask:0xf bank_mask:0xf\n\tv_add_f32_dpp %1, %5, %5 row_ror:4 row_mask:0xf bank_mask:0xf\n\t"
                     "v_add_f32_dpp %2, %6, %6 row_ror:4 row_mask:0xf bank_mask:0xf\n\tv_add_f32_dpp %3, %7, %7 row_ror:4 row_mask:0xf bank_mask:0xf\n\t"
                     "v_add_f32_dpp %0, %0, %0 row_ror:8 row_mask:0xf bank_mask:0xf\n\tv_add_f32_dpp %1, %1, %1 row_ror:8 row_mask:0xf bank_mask:0xf\n\t"
                     "v_add_f32_dpp %2, %2, %2 row_ror:8 row_mask:0xf bank_mask:0xf\n\tv_add_f32_dpp %3, %3, %3 row_ror:8 row_mask:0xf bank_mask:0xf"
                     : "=&v"(e0), "=&v"(e1), "=&v"(e2), "=&v"(e3) : "v"(d0), "v"(d1), "v"(d2), "v"(d3));
        d = f32x4{e0, e1, e2, e3};
        const bool nb = __builtin_isunordered(e0, e1) || __builtin_isunordered(e2, e3);
        return __any(nb);
      };
      if (__builtin_expect(reduce(), 0)) {
#pragma unroll 1
        for (int it = 0; ; ++it) {
          const bool miss = (y0[0] == ~0u) | (y0[1] == ~0u) | (y0[2] == ~0u) | (y0[3] == ~0u) | (y1[0] == ~0u) | (y1[1] == ~0u) | (y1[2] == ~0u) | (y1[3] == ~0u);
          if (!__any(miss)) break;
          if (it >= cap) { cap = 0; break; }
          T += tQ;
          if (!reduce()) break;
        }
      }
      if (((lane >> 2) & 3) < NCOPY && lane < 32) st8(rws, mw, (s + 1) * 16384, u32x2{nosent(pk2(d[0], d[1])), nosent(pk2(d[2], d[3]))});
      T += sw & 255;
      PT(7);
    } else if (w == j) {
      mem_kv(af, sm, rws, LN(), qe_r, idx, s * 32 + h, j);
      if (!KV_DEFER) mem_kv_flag(ws, LN(), s * 32 + h, j, tag);
    } else if (w == wflag) {
      if (s >= 1 && s + 1 < NSEG - 1 && LN() < 4) GLDS1(smb + R_SW + (cb ^ 1) * 128, vl16, ws + sched + (ri >> 5) * 128 + (u32)(s + 1) * 16384, 0, SC1);
      const bool nf = s + 2 < NSEG - 1;
      if (nf && LN() == 0) GLDS1(smb + R_FL, vl16, ws + WS_FLAG + (size_t)(F_KV + (s + 2) * 32 + h) * 128, 0, SC1);
      if (s + 1 < NSEG - 1) {
        while ((int)rfl(*(const volatile u32 LDS3*)(sm + R_VF)) != s + 1) { }
        vx_wo(sm, LN(), 0, 2, cb ? R_VO : R_V2);
      }
      wait_vm0();
      if (nf && __any(*(const volatile u64 LDS3*)(sm + R_FL) != tag)) wait_flags(ws, F_KV + (s + 2) * 32 + h, 1, tag, LN(), cap);
    } else {
      if (s + 1 < NSEG - 1) {
        WAIT_VM(1);
        *(volatile u32 LDS3*)(sm + R_VF) = s + 1;
        vx_wo(sm, LN(), 2, 4, cb ? R_VO : R_V2);
      }
      if ((ri & 31) == 0) {
        const u32 rn = rt32(), cn = (u32)__builtin_amdgcn_s_memtime();
        u32 va, vb;
        {
          float f0, f1;
          asm volatile("v_cvt_f32_u32 %0, %4\n\tv_cvt_f32_u32 %1, %5\n\tv_rcp_f32 %1, %1\n\ts_nop 1\n\tv_mul_f32 %0, %0, %1\n\t"
                       "v_cvt_f32_u32 %1, %6\n\tv_mul_f32 %2, %0, %1\n\tv_cvt_f32_u32 %1, %7\n\tv_mul_f32 %3, %0, %1\n\t"
                       "v_ceil_f32 %2, %2\n\tv_ceil_f32 %3, %3\n\tv_cvt_u32_f32 %2, %2\n\tv_cvt_u32_f32 %3, %3"
                       : "=&v"(f0), "=&v"(f1), "=&v"(va), "=&v"(vb) : "s"(rn - ra_), "s"(cn - ca_), "s"(tCA), "s"(tCB));
        }
        if ((s & 3) == 3) { ra_ = rb_; ca_ = cb_; rb_ = rn; cb_ = cn; }
        run = (s >= 2 && rn - rlast > len + tQ / 2) ? run + 1 : 0;
        rlast = rn;
        if (hold > 0) --hold;
        else if (run >= T_RUN) { extra = min(extra + bump, (u32)T_XMAX); bump = min(2 * bump, 8u); hold = 2; }
        const u32 na = min((u32)rfl(va) + extra, 240u), nb = min((u32)rfl(vb) + (extra + 2) / 3, 120u);
        const u32 nx = (na << 8) | nb;
        len = na + nb;
        if (LN() < 4 && s + 2 < NSEG - 1) st16(rws, sched + LN() * 128 + (ri >> 5) * 16, (s + 2) * 16384, u32x4{nx, nx, nx, nx});
      }
    }
  }
  TLM(blockIdx.x, 5);
  {
    constexpr int s = NSEG - 1;
    f32x4 ak[2];
    u32x4 af[8];
    if (KV_DEFER && w == j) mem_kv_flag(ws, lane, (s - 1) * 32 + h, j, tag);
#pragma unroll 1
    for (int it = 0; ; ++it) {
      if (w == wred) {
        wait_until(T, tQ);
        const char* src = ws + memr + (u32)s * 16384;
        GLDS4(smb + R_M, vl16, src, SC1);
        GLDS4(smb + R_M + 4096, vl16, src + 4096, SC1);
        wait_vm0();
      }
      bar();
#pragma unroll
      for (int k = 0; k < 8; ++k) af[k] = *(const u32x4 LDS3*)(sm + R_M + k * 1024 + vl16);
      if (!__any(any_ones(af))) break;
      if (it >= capB) break;
      T += tQ;
      bar();
    }
    ak[0] = ak[1] = f32x4{0, 0, 0, 0};
    asm volatile("s_nop 1" : "+v"(ak[0]), "+v"(ak[1]));
#pragma unroll
    for (int k = 0; k < 8; ++k) { SMF_VA(ak[0], af[k], wk[0][k], idx); SMF_VA(ak[1], af[k], wk[1][k], idx); }
    MFWAIT2(ak[0], ak[1]);
    float ks_[4];
#pragma unroll
    for (int r = 0; r < 4; ++r) { float a = ak[0][r], b = ak[1][r]; swap32f(a, b); ks_[r] = a + b; }
    *(u32x2 LDS3*)(sm + R_KE + qe_w) = u32x2{pk2(ks_[0], ks_[1]), pk2(ks_[2], ks_[3])};
    bar();
    if (w == j) { mem_kv(af, sm, rws, lane, qe_r, idx, s * 32 + h, j); mem_kv_flag(ws, lane, s * 32 + h, j, tag); }
  }
}
}
#define X_KVM 0
#define X_EARLY 0
namespace mk {

constexpr int X_H = 0, X_QKV = 65536, X_PF = X_QKV + 3 * 24576;
DI void x_role(const Params& P, int h, int par, u64 tag, char LDS3* sm) {
  const int tid = threadIdx.x, lane = tid & 63, w = rfl(tid >> 6);
  const int n = lane & 15, g = lane >> 4, q4 = n >> 2, p4 = n & 3;
  char* ws = P.ws;
  const u32 smb = lds_addr(sm);
  const u32 vl16 = lane * 16;
  int cap = SPIN_CAP;
  u32x4 wq[2][16], wk[2][16], wv[2][16];
  {
    const u32 voff = g * 8192 + ((n ^ ((g << 2) | w)) << 4);
    const char* b0 = (const char*)P.Wq + h * 256;
    const char* b1 = (const char*)P.Wk + h * 256;
    const char* b2 = (const char*)P.Wv + h * 256;
    auto issue = [&](int s, int buf) {
#pragma unroll
      for (int m = 0; m < 4; ++m) {
        const int i = w + 4 * m;
        const size_t go = (size_t)(64 * s + 4 * i) * 8192;
        const u32 lo = smb + buf * 49152 + i * 1024;
        GLDS1(lo, voff, b0 + go, 0, NOSC);
        GLDS1(lo + 16384, voff, b1 + go, 0, NOSC);
        GLDS1(lo + 32768, voff, b2 + go, 0, NOSC);
      }
    };
    issue(0, 0); issue(1, 1); issue(2, 2);
#pragma unroll
    for (int s = 0; s < 8; ++s) {
      if (s <= 5) WAIT_VM(24);
      else if (s == 6) WAIT_VM(12);
      else WAIT_VM(0);
      bar();
      const char LDS3* bp = sm + (s % 3) * 49152;
#pragma unroll
      for (int t = 0; t < 2; ++t)
#pragma unroll
        for (int ks = 0; ks < 2; ++ks) {
          const u32 o0 = img_off(32 * ks + 8 * g + q4, 4 * w + p4) + 8 * t, o1 = img_off(32 * ks + 8 * g + 4 + q4, 4 * w + p4) + 8 * t;
          wq[t][2 * s + ks] = cat4(trd2(bp + o0), trd2(bp + o1));
          wk[t][2 * s + ks] = cat4(trd2(bp + 16384 + o0), trd2(bp + 16384 + o1));
          wv[t][2 * s + ks] = cat4(trd2(bp + 32768 + o0), trd2(bp + 32768 + o1));
          asm volatile("" : "+a"(wq[t][2 * s + ks]), "+a"(wk[t][2 * s + ks]), "+v"(wv[t][2 * s + ks]));
        }
      bar();
      if (s + 3 < 8) issue(s + 3, s % 3);
    }
  }
  const rsrc_t rws = mkrsrc(ws);
  const float SCALE = 0.08838834764831845f, LOG2E = 1.4426950408889634f;
  const int mtB = w & 1, eh = w >> 1;
  const int tq = pm(n, mtB);
  TLM(blockIdx.x, 2);
  bool hall = false;
  auto wait_h = [&](int need) {
    int it = 0;
#pragma unroll 1
    for (; it < cap; ++it) {
      cbar();
      const bool ok = flag_get(ws, F_H + (par + 2 * (lane >> 1)) * 8 + (lane & 1)) == tag;
      if (__all(ok || (lane >> 1) > need)) { hall = __all(ok); break; }
      __builtin_amdgcn_s_sleep(1);
    }
    if (it >= cap) cap = 1;
    cbar();
  };
  wait_h(0);
  GLDS4(smb + X_H + w * 8192, vl16, ws + WS_H + (size_t)par * 32768 + w * 8192, SC1);
  GLDS4(smb + X_H + w * 8192 + 4096, vl16, ws + WS_H + (size_t)par * 32768 + w * 8192 + 4096, SC1);
  wait_vm0();
  bar();

  int ring = 0;
#pragma unroll 1
  for (int it = 0; it < 34; ++it) {
    u64 fk = tag;
    if (it >= 2) fk = flag_get(ws, F_KM + ((par + 2 * (it - 2)) * 32 + h) * 4 + (lane & 3));
    u32x4 km[4]; u32x2 vm[4];
    auto load_kvm = [&](int s) __attribute__((always_inline)) {
#pragma unroll
      for (int k = 0; k < 4; ++k) {
        km[k] = ld16(rws, WS_KM + ((s * 32 + h) * 4 + k) * 512 + (g * 8 + (n & 7)) * 16, 0);
        vm[k] = ld8(rws, WS_VM + ((s * 32 + h) * 8 + 4 * eh + k) * 512 + lane * 8, 0);
      }
    };
    bool early = false;
    if (it < 32) {
      const int s = par + 2 * it;
      if (it + 1 < 32) {
        if (!hall) wait_h(it + 1);
        const u32 lo = smb + X_H + ((it + 1) & 1) * 32768 + w * 8192;
        const char* src = ws + WS_H + (size_t)(s + 2) * 32768 + w * 8192;
        GLDS4(lo, vl16, src, SC1);
        GLDS4(lo + 4096, vl16, src + 4096, SC1);
      }
      const char LDS3* hb = sm + X_H + (it & 1) * 32768 + vl16;
      f32x4 aq[2][2], ak[2][2], av[2][2];
      u32x4 h0 = *(const u32x4 LDS3*)hb, h1 = *(const u32x4 LDS3*)(hb + 16384);
#pragma unroll
      for (int ks = 0; ks < 16; ++ks) {
        u32x4 n0 = h0, n1 = h1;
        if (ks + 1 < 16) { n0 = *(const u32x4 LDS3*)(hb + (ks + 1) * 1024); n1 = *(const u32x4 LDS3*)(hb + 16384 + (ks + 1) * 1024); }
        if (ks == 0) {
#pragma unroll
          for (int t = 0; t < 2; ++t) {
            MF32_A0(aq[t][0], wq[t][0], h0); MF32_A0(aq[t][1], wq[t][0], h1);
            MF32_A0(ak[t][0], wk[t][0], h0); MF32_A0(ak[t][1], wk[t][0], h1);
            MF32_0(av[t][0], h0, wv[t][0]);  MF32_0(av[t][1], h1, wv[t][0]);
          }
        } else {
#pragma unroll
          for (int t = 0; t < 2; ++t) {
            MF32_A(aq[t][0], wq[t][ks], h0); MF32_A(aq[t][1], wq[t][ks], h1);
            MF32_A(ak[t][0], wk[t][ks], h0); MF32_A(ak[t][1], wk[t][ks], h1);
            MF32(av[t][0], h0, wv[t][ks]);   MF32(av[t][1], h1, wv[t][ks]);
          }
        }
        h0 = n0; h1 = n1;
      }
      asm volatile("s_nop 7" : "+v"(aq[0][0]), "+v"(aq[0][1]), "+v"(aq[1][0]), "+v"(aq[1][1]), "+v"(ak[0][0]), "+v"(ak[0][1]), "+v"(ak[1][0]), "+v"(ak[1][1]),
                   "+v"(av[0][0]), "+v"(av[0][1]), "+v"(av[1][0]), "+v"(av[1][1]));
      if (X_KVM && it >= 2) {
        asm volatile("" : "+v"(fk));
        early = __all(fk == tag);
        if (early) load_kvm(par + 2 * (it - 2));
      }
      char LDS3* qb = sm + X_QKV + ring * 24576;
      const u32 go = (u32)(s * 32 + h) * 8192;
#pragma unroll
      for (int mt = 0; mt < 2; ++mt) {
        const u32x4 qv = cat4(pk4(aq[0][mt]), pk4(aq[1][mt])), kv = cat4(pk4(ak[0][mt]), pk4(ak[1][mt]));
        *(u32x4 LDS3*)(qb + (mt * 4 + w) * 1024 + vl16) = qv;
        *(u32x4 LDS3*)(qb + 8192 + (mt * 4 + w) * 1024 + vl16) = kv;
        if (s < NSEG - 1) st16(rws, WS_KX + go + (mt * 4 + w) * 1024 + vl16, 0, kv);
      }
#pragma unroll
      for (int t = 0; t < 2; ++t) {
        const u32x4 vv = cat4(pk4(av[t][0]), pk4(av[t][1]));
        *(u32x4 LDS3*)(qb + 16384 + (2 * w + t) * 1024 + vl16) = vv;
        if (s < NSEG - 1) st16(rws, WS_VX + go + (2 * w + t) * 1024 + vl16, 0, vv);
      }
      if (it < 2) {
        wait_vm0();
        bar();
        flag_set(ws, F_KV + s * 32 + h, tag, tid);
        if (h == 0) TLS(1, s);
      }
    }
    ring = ring == 2 ? 0 : ring + 1;
    if (it >= 2) {
      const int s = par + 2 * (it - 2);
      const char LDS3* qb = sm + X_QKV + ring * 24576 + vl16;
      u32x4 qf[4], k0[4], k1[4], vx[4];
#pragma unroll
      for (int k = 0; k < 4; ++k) {
        qf[k] = *(const u32x4 LDS3*)(qb + (mtB * 4 + k) * 1024);
        k0[k] = *(const u32x4 LDS3*)(qb + 8192 + k * 1024);
        k1[k] = *(const u32x4 LDS3*)(qb + 8192 + 4096 + k * 1024);
        vx[k] = *(const u32x4 LDS3*)(qb + 16384 + (4 * eh + k) * 1024);
      }
      f32x4 sA, sB, sC;
      MF32_0(sB, k0[0], qf[0]); MF32_0(sC, k1[0], qf[0]);
#pragma unroll
      for (int k = 1; k < 4; ++k) { MF32(sB, k0[k], qf[k]); MF32(sC, k1[k], qf[k]); }
      MFWAIT2(sB, sC);
      if (X_EARLY && it < 32) {
        wait_vm0();
        bar();
        if (s + 4 < NSEG - 1) flag_set(ws, F_KV + (s + 4) * 32 + h, tag, tid);
        if (h == 0) TLS(1, s + 4);
      }
      asm volatile("" : "+v"(fk));
      if (!early) {
        if (!__all(fk == tag)) wait_flags(ws, F_KM + (s * 32 + h) * 4, 4, tag, lane, cap);
        load_kvm(s);
      }
      MF32_0(sA, km[0], qf[0]);
#pragma unroll
      for (int k = 1; k < 4; ++k) MF32(sA, km[k], qf[k]);
      MFWAIT3(sA, sB, sC);
      const float NEG = -3.0e38f;
#pragma unroll
      for (int r = 0; r < 4; ++r) {
        sA[r] = rbf(rbf(sA[r]) * SCALE);
        sB[r] = (8 * g + r <= tq) ? rbf(rbf(sB[r]) * SCALE) : NEG;
        sC[r] = (8 * g + 4 + r <= tq) ? rbf(rbf(sC[r]) * SCALE) : NEG;
      }
      float mx = fmaxf(fmaxf(fmaxf(sA[0], sA[1]), fmaxf(sA[2], sA[3])), fmaxf(fmaxf(fmaxf(sB[0], sB[1]), fmaxf(sB[2], sB[3])), fmaxf(fmaxf(sC[0], sC[1]), fmaxf(sC[2], sC[3]))));
      mx = gmax4(mx);
      float pA[4], pB[4], pC[4];
#pragma unroll
      for (int r = 0; r < 4; ++r) {
        pA[r] = __builtin_amdgcn_exp2f((sA[r] - mx) * LOG2E);
        pB[r] = (8 * g + r <= tq) ? __builtin_amdgcn_exp2f((sB[r] - mx) * LOG2E) : 0.f;
        pC[r] = (8 * g + 4 + r <= tq) ? __builtin_amdgcn_exp2f((sC[r] - mx) * LOG2E) : 0.f;
      }
      float ls = 0.5f * ((pA[0] + pA[1]) + (pA[2] + pA[3])) + ((pB[0] + pB[1]) + (pB[2] + pB[3])) + ((pC[0] + pC[1]) + (pC[2] + pC[3]));
      ls = gsum4(ls);
      const float rl = __builtin_amdgcn_rcpf(ls);
      u32x2 pa = {pk2(pA[0] * rl, pA[1] * rl), pk2(pA[2] * rl, pA[3] * rl)};
      u32x4 px = {pk2(pB[0] * rl, pB[1] * rl), pk2(pB[2] * rl, pB[3] * rl), pk2(pC[0] * rl, pC[1] * rl), pk2(pC[2] * rl, pC[3] * rl)};
      asm volatile("s_nop 1" : "+v"(pa), "+v"(px));
      f32x4 o[4];
#pragma unroll
      for (int k = 0; k < 4; ++k) MF32_0(o[k], vx[k], px);
#pragma unroll
      for (int k = 0; k < 4; ++k) MF16(o[k], vm[k], pa);
      MFWAIT4(o[0], o[1], o[2], o[3]);
#pragma unroll
      for (int k = 0; k < 2; ++k) {
        st16(rws, WS_O + (u32)((s * 2 + mtB) * 128 + h * 4 + 2 * eh + k) * 1024 + vl16, 0, cat4(pk4(o[2 * k]), pk4(o[2 * k + 1])));
      }
      wait_vm0();
      GLDS2(smb + X_PF + w * 2048, vl16, ws + WS_O + (size_t)((s * 2 + mtB) * 128 + h * 4 + 2 * eh) * 1024, SC1);
      bar();
      flag_set(ws, F_O + s * 32 + h, tag, tid);
      if (!X_EARLY && it < 32 && s + 4 < NSEG - 1) flag_set(ws, F_KV + (s + 4) * 32 + h, tag, tid);
      if (!X_EARLY && h == 0 && it < 32) TLS(1, s + 4);
      if (h == 0) TLS(2, s);
    }
  }
}

DI void mod_job(const Params& P, int gi, u64 tag, char LDS3* sm) {
  const int tid = threadIdx.x, lane = tid & 63, w = tid >> 6;
  float LDS3* sc = (float LDS3*)sm;
  float LDS3* red = (float LDS3*)(sm + 2048);
  const int rg = (lane >> 3) + 8 * w, ch = lane & 7;
  u32x4 wv[16];
  const char* wp = (const char*)P.wmod + (size_t)rg * 6144 + gi * 128 + ch * 16;
#pragma unroll
  for (int i = 0; i < 16; ++i) wv[i] = *(const u32x4*)(wp + (size_t)i * 32 * 6144);
  {
    const u32 cc = ((const u32*)P.c)[tid];
    const float c0 = blo(cc), c1 = bhi(cc);
    sc[2 * tid]     = rbf(c0 * __builtin_amdgcn_rcpf(1.f + __expf(-c0)));
    sc[2 * tid + 1] = rbf(c1 * __builtin_amdgcn_rcpf(1.f + __expf(-c1)));
  }
  bar();
  float acc[8] = {0, 0, 0, 0, 0, 0, 0, 0};
#pragma unroll
  for (int i = 0; i < 16; ++i) {
    const float s = sc[rg + 32 * i];
#pragma unroll
    for (int k = 0; k < 4; ++k) { acc[2 * k] += s * blo(wv[i][k]); acc[2 * k + 1] += s * bhi(wv[i][k]); }
  }
  *(f32x4 LDS3*)(red + rg * 64 + ch * 8)     = f32x4{acc[0], acc[1], acc[2], acc[3]};
  *(f32x4 LDS3*)(red + rg * 64 + ch * 8 + 4) = f32x4{acc[4], acc[5], acc[6], acc[7]};
  bar();
  if (tid < 64) {
    float t = 0;
#pragma unroll
    for (int r = 0; r < 32; ++r) t += red[r * 64 + tid];
    __hip_atomic_store((float*)(P.ws + WS_MOD) + gi * 64 + tid, rbf(t), __ATOMIC_RELAXED, __HIP_MEMORY_SCOPE_AGENT);
    wait_vm0();
    flag_set(P.ws, F_MOD + gi, tag, tid);
    { char* ws = P.ws; TLM(blockIdx.x, 6); (void)ws; }
  }
  bar();
}

#define O_WK 0
DI void o_role(const Params& P, int kq, int nq, u64 tag, char LDS3* sm) {
  const int tid = threadIdx.x, lane = tid & 63, w = rfl(tid >> 6);
  const int n = lane & 15, g = lane >> 4, q4 = n >> 2, p4 = n & 3;
  char* ws = P.ws;
  const u32 smb = lds_addr(sm);
  const u32 vl16 = lane * 16;
  int cap = SPIN_CAP;
  u32x4 wo[2][32];
  {
    const u32 voff = g * 1024 + ((n ^ ((g << 2) | w)) << 4);
    const char* b0 = (const char*)P.Wo + (size_t)kq * 1024 * 1024 + nq * 256;
#pragma unroll
    for (int hf = 0; hf < 2; ++hf) {
#pragma unroll
      for (int m = 0; m < 32; ++m) {
        const int i = w + 4 * m;
        GLDS1(smb + i * 1024, voff, b0 + (size_t)(512 * hf + 4 * i) * 1024, 0, NOSC);
      }
      wait_vm0(); bar();
#pragma unroll
      for (int ks = 0; ks < 16; ++ks)
#pragma unroll
        for (int t = 0; t < 2; ++t) {
          const u32 o0 = img_off(32 * ks + 8 * g + q4, 4 * w + p4) + 8 * t, o1 = img_off(32 * ks + 8 * g + 4 + q4, 4 * w + p4) + 8 * t;
          wo[t][16 * hf + ks] = cat4(trd2(sm + o0), trd2(sm + o1));
          asm volatile("" : "+a"(wo[t][16 * hf + ks]));
        }
      bar();
    }
  }
  const rsrc_t rws = mkrsrc(ws);
  auto fetch = [&](int s) {
    const u32 lo = smb + (s & 1) * 65536;
#pragma unroll
    for (int c = 0; c < 4; ++c) {
      const char* src = ws + WS_O + ((size_t)(s * 2 + (w >> 1)) * 128 + 32 * kq + 8 * c + 4 * (w & 1)) * 1024;
      GLDS4(lo + ((w >> 1) * 32 + 8 * c + 4 * (w & 1)) * 1024, vl16, src, SC1);
    }
  };
  bool have = false;
  u32 LDS3* okw = (u32 LDS3*)(sm + 131072);
  OT0();
#pragma unroll 1
  for (int s = 0; s < NSEG; ++s) {
    const bool late = have;
    if (!have) { wait_flags(ws, F_O + s * 32 + 8 * kq, 8, tag, lane, cap); OT(0); fetch(s); }
    const u64 nxt = flag_get(ws, F_O + min(s + 1, NSEG - 1) * 32 + 8 * kq + (lane & 7));
    const char LDS3* ob = sm + (s & 1) * 65536 + vl16;
    f32x4 acc[2][2];
    u32x4 b0[2][8], b1[2][8];
    OT(1);
    WAIT_VM(13);
    bar();
    OT(2);
    if (late) flag_set(ws, F_OP + (s - 1) * 16 + kq * 4 + nq, tag, tid);
#pragma unroll
    for (int k = 0; k < 8; ++k) { b0[0][k] = *(const u32x4 LDS3*)(ob + k * 1024); b1[0][k] = *(const u32x4 LDS3*)(ob + (32 + k) * 1024); }
    SB();
#pragma unroll
    for (int c = 0; c < 4; ++c) {
#pragma unroll
      for (int k = 0; k < 8; ++k) {
        const int ks = 8 * c + k;
        if (k == O_WK && c < 3) {
          OT(3);
          if (c == 0) WAIT_VM(9); else if (c == 1) WAIT_VM(5); else WAIT_VM(0);
          if (c == 2) { const bool ok = __all(nxt == tag); if (lane == 0) okw[w] = ok; }
          bar();
          OT(5);
          if (c == 2) have = s + 1 < NSEG && (okw[0] & okw[1] & okw[2] & okw[3]) != 0;
          SB();
        }
        if (ks == 0) { MF32_A0(acc[0][0], wo[0][0], b0[0][0]); MF32_A0(acc[1][0], wo[1][0], b0[0][0]); MF32_A0(acc[0][1], wo[0][0], b1[0][0]); MF32_A0(acc[1][1], wo[1][0], b1[0][0]); }
        else { MF32_A(acc[0][0], wo[0][ks], b0[c & 1][k]); MF32_A(acc[1][0], wo[1][ks], b0[c & 1][k]); MF32_A(acc[0][1], wo[0][ks], b1[c & 1][k]); MF32_A(acc[1][1], wo[1][ks], b1[c & 1][k]); }
        if (c < 3 && k >= O_WK) {
#pragma unroll
          for (int q = (k - O_WK) * 8 / (8 - O_WK); q < (k + 1 - O_WK) * 8 / (8 - O_WK); ++q) {
            b0[(c + 1) & 1][q] = *(const u32x4 LDS3*)(ob + (8 * c + 8 + q) * 1024); b1[(c + 1) & 1][q] = *(const u32x4 LDS3*)(ob + (40 + 8 * c + q) * 1024);
          }
        }
        SB();
      }
    }
    MFWAIT4(acc[0][0], acc[0][1], acc[1][0], acc[1][1]);
#pragma unroll
    for (int mt = 0; mt < 2; ++mt)
      st16(rws, WS_OP + (u32)(((s * 4 + kq) * 2 + mt) * 16 + 4 * nq + w) * 1024 + vl16, 0, cat4(pk4(acc[0][mt]), pk4(acc[1][mt])));
    if (kq == 0 && nq == 0) TLS(3, s);
    OT(3);
    if (have) fetch(s + 1);
    else { wait_vm0(); bar(); flag_set(ws, F_OP + s * 16 + kq * 4 + nq, tag, tid); }
    OT(4);
  }
  OTW(kq * 4 + nq);
}

DI float ln_rden(float var) { return __builtin_amdgcn_rcpf(rbf(__builtin_amdgcn_sqrtf(rbf(rbf(var) + 1e-5f)))); }
DI u32 ln_out2(float a, float b, float mb, float rden, float ca, float cb, float sa, float sb) {
  u32 t = pk2(a - mb, b - mb);
  t = pk2(blo(t) * rden, bhi(t) * rden);
  t = pk2(blo(t) * ca, bhi(t) * cb);
  return pk2(blo(t) + sa, bhi(t) + sb);
}
DI void l_role(const Params& P, int li, u64 tag, char LDS3* sm) {
  const int tid = threadIdx.x, lane = tid & 63, w = rfl(tid >> 6);
  const int n = lane & 15, g = lane >> 4;
  char* ws = P.ws;
  const u32 vl16 = lane * 16;
  int cap = SPIN_CAP;
  const rsrc_t rws = mkrsrc(ws);
  float LDS3* md = (float LDS3*)sm;
  float LDS3* st = (float LDS3*)(sm + 10240);
  bool have_mod = false;
#pragma unroll 1
  for (int r = 0; r < 4; ++r) {
    const int i = li * 4 + w + 32 * r, s = i >> 1, mt = i & 1;
    const char* xp = (const char*)P.x + (size_t)(s * 32 + pm(n, mt)) * 1024 + g * 16;
    u32x4 xv[16];
#pragma unroll
    for (int ks = 0; ks < 16; ++ks) xv[ks] = *(const u32x4*)(xp + ks * 64);
    float sum = 0;
#pragma unroll
    for (int ks = 0; ks < 16; ++ks)
#pragma unroll
      for (int k = 0; k < 4; ++k) sum += blo(xv[ks][k]) + bhi(xv[ks][k]);
    const float mean = gsum4(sum) * (1.f / 512.f);
    float m2 = 0;
#pragma unroll
    for (int ks = 0; ks < 16; ++ks)
#pragma unroll
      for (int k = 0; k < 4; ++k) { const float a = blo(xv[ks][k]) - mean, b = bhi(xv[ks][k]) - mean; m2 += a * a + b * b; }
    const float rden = ln_rden(gsum4(m2) * (1.f / 512.f)), mb = rbf(mean);
    if (!have_mod) {
      if (w == 0) wait_flags(ws, F_MOD, 40, tag, lane, cap);
      bar();
      for (int q = tid; q < 640; q += 256) {
        const u32x4 v = ld16(rws, WS_MOD + q * 16, 0);
        f32x4 f = __builtin_bit_cast(f32x4, v);
        const int part = q >> 7;
        if (part == 1 || part == 4) { f[0] = rbf(1.f + f[0]); f[1] = rbf(1.f + f[1]); f[2] = rbf(1.f + f[2]); f[3] = rbf(1.f + f[3]); }
        *(f32x4 LDS3*)(md + q * 4) = f;
      }
      bar();
      have_mod = true;
    }
#pragma unroll
    for (int ks = 0; ks < 16; ++ks) {
      const int c = 32 * ks + 8 * g;
      const f32x4 s0 = *(const f32x4 LDS3*)(md + c), s1 = *(const f32x4 LDS3*)(md + c + 4);
      const f32x4 c0 = *(const f32x4 LDS3*)(md + 512 + c), c1 = *(const f32x4 LDS3*)(md + 512 + c + 4);
      u32x4 o;
      o[0] = ln_out2(blo(xv[ks][0]), bhi(xv[ks][0]), mb, rden, c0[0], c0[1], s0[0], s0[1]);
      o[1] = ln_out2(blo(xv[ks][1]), bhi(xv[ks][1]), mb, rden, c0[2], c0[3], s0[2], s0[3]);
      o[2] = ln_out2(blo(xv[ks][2]), bhi(xv[ks][2]), mb, rden, c1[0], c1[1], s1[0], s1[1]);
      o[3] = ln_out2(blo(xv[ks][3]), bhi(xv[ks][3]), mb, rden, c1[2], c1[3], s1[2], s1[3]);
      st16(rws, WS_H + (u32)(i * 16 + ks) * 1024 + vl16, 0, o);
    }
    wait_vm0();
    flag_set(ws, F_H + s * 8 + mt, tag, lane);
    if (i == 0) TLS(7, 0);
    if (i == 127 && lane == 0) ((u64*)(ws + WS_END + (7 << 20)))[7 * 64 + 1] = __builtin_amdgcn_s_memrealtime();
  }
  OT0();
#pragma unroll 1
  for (int r = 0; r < 16; ++r) {
    const int i = li + 8 * r, s = i >> 1, mt = i & 1;
    const char* xp = (const char*)P.x + (size_t)(s * 32 + pm(n, mt)) * 1024 + w * 256 + g * 16;
    u32x4 xv[4], pv[4][4];
#pragma unroll
    for (int k = 0; k < 4; ++k) xv[k] = *(const u32x4*)(xp + k * 64);
    wait_flags(ws, F_OP + s * 16, 16, tag, lane, cap);
    OT(0);
#pragma unroll
    for (int kq = 0; kq < 4; ++kq)
#pragma unroll
      for (int k = 0; k < 4; ++k) pv[kq][k] = ld16(rws, WS_OP + (u32)(((s * 4 + kq) * 2 + mt) * 16 + 4 * w + k) * 1024 + vl16, 0);
    float x1[32];
    u32x4 xo[4];
    float sum = 0;
#pragma unroll
    for (int k = 0; k < 4; ++k) {
      const int c = 128 * w + 32 * k + 8 * g;
      const f32x4 g0 = *(const f32x4 LDS3*)(md + 1024 + c), g1 = *(const f32x4 LDS3*)(md + 1024 + c + 4);
#pragma unroll
      for (int q = 0; q < 4; ++q) {
        const float alo = rbf((blo(pv[0][k][q]) + blo(pv[1][k][q])) + (blo(pv[2][k][q]) + blo(pv[3][k][q])));
        const float ahi = rbf((bhi(pv[0][k][q]) + bhi(pv[1][k][q])) + (bhi(pv[2][k][q]) + bhi(pv[3][k][q])));
        const float glo = q < 2 ? g0[2 * q] : g1[2 * q - 4], ghi = q < 2 ? g0[2 * q + 1] : g1[2 * q - 3];
        const u32 ga2 = pk2(glo * alo, ghi * ahi);
        const u32 pr = pk2(blo(xv[k][q]) + blo(ga2), bhi(xv[k][q]) + bhi(ga2));
        xo[k][q] = pr;
        x1[8 * k + 2 * q] = blo(pr); x1[8 * k + 2 * q + 1] = bhi(pr);
        sum += blo(pr) + bhi(pr);
      }
      st16(rws, WS_X1 + (u32)(i * 16 + 4 * w + k) * 1024 + vl16, 0, xo[k]);
    }
    const float mw = gsum4(sum) * (1.f / 128.f);
    float m2 = 0;
#pragma unroll
    for (int q = 0; q < 32; ++q) { const float d = x1[q] - mw; m2 += d * d; }
    m2 = gsum4(m2);
    OT(2);
    float LDS3* sb = st + (r & 1) * 128;
    if (g == 0) *(f32x2 LDS3*)(sb + (w * 16 + n) * 2) = f32x2{mw, m2};
    bar();
    float mean = 0, M2 = 0;
    {
      f32x2 t[4];
#pragma unroll
      for (int q = 0; q < 4; ++q) { t[q] = *(const f32x2 LDS3*)(sb + (q * 16 + n) * 2); mean += t[q][0]; }
      mean *= 0.25f;
#pragma unroll
      for (int q = 0; q < 4; ++q) { const float d = t[q][0] - mean; M2 += t[q][1] + 128.f * d * d; }
    }
    const float rden = ln_rden(M2 * (1.f / 512.f)), mb = rbf(mean);
#pragma unroll
    for (int k = 0; k < 4; ++k) {
      const int c = 128 * w + 32 * k + 8 * g;
      const f32x4 s0 = *(const f32x4 LDS3*)(md + 1536 + c), s1 = *(const f32x4 LDS3*)(md + 1536 + c + 4);
      const f32x4 c0 = *(const f32x4 LDS3*)(md + 2048 + c), c1 = *(const f32x4 LDS3*)(md + 2048 + c + 4);
      u32x4 o;
      o[0] = ln_out2(x1[8 * k + 0], x1[8 * k + 1], mb, rden, c0[0], c0[1], s0[0], s0[1]);
      o[1] = ln_out2(x1[8 * k + 2], x1[8 * k + 3], mb, rden, c0[2], c0[3], s0[2], s0[3]);
      o[2] = ln_out2(x1[8 * k + 4], x1[8 * k + 5], mb, rden, c1[0], c1[1], s1[0], s1[1]);
      o[3] = ln_out2(x1[8 * k + 6], x1[8 * k + 7], mb, rden, c1[2], c1[3], s1[2], s1[3]);
      st16(rws, WS_H2 + (u32)(i * 16 + 4 * w + k) * 1024 + vl16, 0, o);
    }
    OT(3);
    wait_vm0();
    bar();
    flag_set(ws, F_H2 + s * 8 + mt, tag, tid);
    OT(4);
    if (mt == 0) TLS(4, s);
  }
  OTW(56 + li);
}

DI void u_role(const Params& P, int u, u64 tag, char LDS3* sm) {
  const int tid = threadIdx.x, lane = tid & 63, w = rfl(tid >> 6);
  const int n = lane & 15, g = lane >> 4, q4 = n >> 2, p4 = n & 3;
  char* ws = P.ws;
  const u32 smb = lds_addr(sm);
  const u32 vl16 = lane * 16;
  int cap = SPIN_CAP;
  const int mt = w & 1, gl = w >> 1, grp = 2 * u + gl;
  u32x4 wg[2][16], wu[2][16];
  {
    const int ch = n ^ ((g << 2) | w);
    int col = 64 * u + 8 * (ch & 7);
    if (col >= DF) col -= 32;
    const char* src = (const char*)(ch < 8 ? P.wg : P.wu) + (size_t)g * (DF * 2) + col * 2;
#pragma unroll
    for (int m = 0; m < 32; ++m) {
      const int i = w + 4 * m;
      const char* a = src + (size_t)(4 * i) * (DF * 2);
      GLDSV(smb + i * 1024, a, NOSC);
    }
    wait_vm0(); bar();
#pragma unroll
    for (int ks = 0; ks < 16; ++ks)
#pragma unroll
      for (int t = 0; t < 2; ++t) {
        const u32 r0 = 32 * ks + 8 * g + q4, r1 = r0 + 4;
        wg[t][ks] = cat4(trd2(sm + img_off(r0, 4 * gl + p4) + 8 * t), trd2(sm + img_off(r1, 4 * gl + p4) + 8 * t));
        wu[t][ks] = cat4(trd2(sm + img_off(r0, 8 + 4 * gl + p4) + 8 * t), trd2(sm + img_off(r1, 8 + 4 * gl + p4) + 8 * t));
        asm volatile("" : "+a"(wg[t][ks]), "+a"(wu[t][ks]));
      }
    bar();
  }
  const rsrc_t rws = mkrsrc(ws);
  auto fetch = [&](int s) {
#pragma unroll
    for (int c = 0; c < 2; ++c) {
      const u32 o = ((w >> 1) * 16 + 8 * c + 4 * (w & 1)) * 1024;
      GLDS4(smb + (s & 1) * 32768 + o, vl16, ws + WS_H2 + (size_t)s * 32768 + o, SC1);
    }
  };
  bool have = false;
  OT0();
#pragma unroll 1
  for (int s = 0; s < NSEG; ++s) {
    if (!have) { wait_flags(ws, F_H2 + s * 8, 2, tag, lane, cap); OT(0); fetch(s); }
    const u64 nxt = flag_get(ws, F_H2 + min(s + 1, NSEG - 1) * 8 + (lane & 1));
    const char LDS3* hb = sm + (s & 1) * 32768 + mt * 16384 + vl16;
    f32x4 ag[2], au[2];
#pragma unroll
    for (int c = 0; c < 2; ++c) {
      OT(c == 0 ? 1 : 3);
      if (c == 0) WAIT_VM(5); else WAIT_VM(0);
      bar();
      OT(2);
      u32x4 b[8];
#pragma unroll
      for (int k = 0; k < 8; ++k) b[k] = *(const u32x4 LDS3*)(hb + (8 * c + k) * 1024);
      SB();
#pragma unroll
      for (int k = 0; k < 8; ++k) {
        const int ks = 8 * c + k;
        if (ks == 0) { MF32_A0(ag[0], wg[0][0], b[0]); MF32_A0(ag[1], wg[1][0], b[0]); MF32_A0(au[0], wu[0][0], b[0]); MF32_A0(au[1], wu[1][0], b[0]); }
        else { MF32_A(ag[0], wg[0][ks], b[k]); MF32_A(ag[1], wg[1][ks], b[k]); MF32_A(au[0], wu[0][ks], b[k]); MF32_A(au[1], wu[1][ks], b[k]); }
      }
    }
    MFWAIT4(ag[0], ag[1], au[0], au[1]);
    f32x4 a[2];
#pragma unroll
    for (int t = 0; t < 2; ++t)
#pragma unroll
      for (int r = 0; r < 4; ++r) {
        const float x = rbf(ag[t][r]), up = rbf(au[t][r]);
        const float y2 = x * (1.f + 0.044715f * x * x) * (-2.f * 0.7978845608028654f * 1.4426950408889634f);
        const float ge = rbf(x * __builtin_amdgcn_rcpf(1.f + __builtin_amdgcn_exp2f(y2)));
        a[t][r] = ge * up;
      }
    OT(3);
    if (u == 0) TLS(5, s);
    if (grp < 43) st16(rws, WS_ACT + (u32)((s * 2 + mt) * 43 + grp) * 1024 + vl16, 0, cat4(pk4(a[0]), pk4(a[1])));
    have = s + 1 < NSEG && __all(nxt == tag);
    OT(4);
    if (have) { fetch(s + 1); WAIT_VM(8); } else wait_vm0();
    bar();
    flag_set(ws, F_ACT + s * 32 + u, tag, tid);
    OT(5);
  }
  OTW(16 + u);
}

DI void d_role(const Params& P, int d, u64 tag, char LDS3* sm) {
  const int tid = threadIdx.x, lane = tid & 63, w = rfl(tid >> 6);
  const int n = lane & 15, g = lane >> 4, q4 = n >> 2, p4 = n & 3;
  char* ws = P.ws;
  const u32 smb = lds_addr(sm);
  const u32 vl16 = lane * 16;
  int cap = SPIN_CAP;
  const int mt = d & 1, dq = d >> 1, t = w & 1, dd = 2 * dq + (w >> 1);
  u32x4 wd[43];
  {
    const u32 voff = (lane >> 3) * 1024 + (lane & 7) * 16;
    const char* b0 = (const char*)P.wd + dq * 128;
#pragma unroll
    for (int hf = 0; hf < 2; ++hf) {
#pragma unroll
      for (int m = 0; m < 22; ++m) {
        const int i = w + 4 * m;
        if (hf == 0 || m < 21) GLDS1(smb + i * 1024, voff, b0 + (size_t)(704 * hf + 8 * i) * 1024, 0, NOSC);
      }
      wait_vm0(); bar();
#pragma unroll
      for (int k = 0; k < 22; ++k) {
        if (hf == 0 || k < 21) {
          const u32 o = (32 * k + 8 * g + q4) * 128 + (w >> 1) * 64 + p4 * 16 + 8 * t;
          wd[22 * hf + k] = cat4(trd2(sm + o), trd2(sm + o + 512));
          asm volatile("" : "+a"(wd[22 * hf + k]));
        }
      }
      bar();
    }
  }
  const rsrc_t rws = mkrsrc(ws);
  wait_flags(ws, F_MOD + 40 + dq, 1, tag, lane, cap);
  const f32x4 gb = __builtin_bit_cast(f32x4, ld16(rws, WS_MOD + (2560 + 32 * dd + 8 * g + 4 * t) * 4, 0));
  u32x2 x1n = {0, 0};
  auto fetch = [&](int s) {
    x1n = ld8(rws, WS_X1 + (u32)((s * 2 + mt) * 16 + dd) * 1024 + vl16 + 8 * t, 0);
    const u32 lo = smb + (s & 1) * 44032;
    const char* src = ws + WS_ACT + (size_t)(s * 2 + mt) * 43 * 1024;
#pragma unroll
    for (int m = 0; m < 11; ++m) {
      const int ks = m < 10 ? 4 * m + w : (w < 3 ? 40 + w : 39);
      GLDS1(lo + ks * 1024, vl16, src + ks * 1024, 0, SC1);
    }
  };
  auto fidx = [&](int s) { return lane < 22 ? F_ACT + s * 32 + lane : F_H2 + s * 8 + mt; };
  bool have = false;
  OT0();
#pragma unroll 1
  for (int s = 0; s < NSEG; ++s) {
    if (!have) { wait_flags_idx(ws, fidx(s), tag, cap); OT(0); fetch(s); }
    const u64 nxt = flag_get(ws, fidx(min(s + 1, NSEG - 1)));
    f32x4 a0, a1;
    u32x2 x1;
    const char LDS3* ab = sm + (s & 1) * 44032 + vl16;
#pragma unroll
    for (int c = 0; c < 4; ++c) {
      OT(c == 0 ? 1 : 3);
      if (c == 0) WAIT_VM(9); else if (c == 1) WAIT_VM(6); else if (c == 2) WAIT_VM(3); else WAIT_VM(0);
      bar();
      OT(2);
      u32x4 b[12];
#pragma unroll
      for (int k = 0; k < 12; ++k) if (12 * c + k < 43) b[k] = *(const u32x4 LDS3*)(ab + (12 * c + k) * 1024);
      if (c == 3) {
        x1 = x1n;
        asm volatile("" : "+v"(x1));
        have = s + 1 < NSEG && __all(nxt == tag);
        if (have) fetch(s + 1);
      }
      SB();
#pragma unroll
      for (int k = 0; k < 12; ++k) {
        const int ks = 12 * c + k;
        if (ks < 43) {
          if (ks == 0) MF32_A0(a0, wd[0], b[0]);
          else if (ks == 1) MF32_A0(a1, wd[1], b[1]);
          else if (ks & 1) MF32_A(a1, wd[ks], b[k]);
          else MF32_A(a0, wd[ks], b[k]);
        }
      }
    }
    MFWAIT2(a0, a1);
    float y[4];
#pragma unroll
    for (int r = 0; r < 4; ++r) y[r] = rbf(gb[r] * rbf(a0[r] + a1[r]));
    const u32x2 o = {pk2(blo(x1[0]) + y[0], bhi(x1[0]) + y[1]), pk2(blo(x1[1]) + y[2], bhi(x1[1]) + y[3])};
    *(u32x2*)((char*)P.out + (size_t)(s * 32 + pm(n, mt)) * 1024 + (32 * dd + 8 * g + 4 * t) * 2) = o;
    if (d == 0) TLS(6, s);
    OT(4);
  }
  OTW(40 + d);
}

DI void worker_role(const Params& P, int xw, int slot, u64 tag, char LDS3* sm) {
  const int tid = threadIdx.x;
  flag_set(P.ws, F_READY + 128 + xw * 32 + slot, tag, tid);
  if (slot < 16) { x_role(P, xw * 8 + (slot >> 1), slot & 1, tag, sm); return; }
  if (slot == 24 || slot == 25) { l_role(P, xw * 2 + (slot - 24), tag, sm); return; }
  const int u = xw * 6 + (slot - 26);
  int gi = -1;
  if (slot < 24) gi = xw * 8 + (slot - 16);
  else if (slot < 30) gi = 32 + xw * 4 + (slot - 26);
  if (gi >= 0) mod_job(P, gi, tag, sm);
  if (slot < 20) o_role(P, xw, slot - 16, tag, sm);
  else if (slot < 24) d_role(P, xw * 4 + (slot - 20), tag, sm);
  else if (u < 22) u_role(P, u, tag, sm);
  else if (u == 22) {
    int cap = SPIN_CAP;
    if (tid < 64) {
      for (int q = 0; q < 4; ++q) wait_votes(P.ws, F_READY + 64 * q, tag, tid, cap);
      if (tid == 0) __hip_atomic_store((u64*)(P.ws + WS_NONCE), tag, __ATOMIC_RELAXED, __HIP_MEMORY_SCOPE_AGENT);
    }
  }
}
}
namespace mk {
__global__ void __launch_bounds__(256, 1) mega_kernel(Params P) {
  extern __shared__ __attribute__((aligned(16))) char smem_raw[];
  char LDS3* sm = (char LDS3*)smem_raw;
  const int b = blockIdx.x, xcd = b & 7, slot = b >> 3;
  const u64 nonce = __hip_atomic_load((const u64*)(P.ws + WS_NONCE), __ATOMIC_RELAXED, __HIP_MEMORY_SCOPE_AGENT);
  const u64 tagv = make_tag(nonce);
  const u64 tag = ((u64)(u32)rfl((int)(u32)(tagv >> 32)) << 32) | (u32)rfl((int)(u32)tagv);
  if (xcd < 4) {
    rec_role(P, xcd * 32 + slot, tag, sm);
  } else {
    worker_role(P, (xcd - 4), slot, tag, sm);
  }
}
}

extern "C" void kernel_launch(void* const* d_in, const int* in_sizes, int n_in, void* d_out, int out_size, void* d_ws, size_t ws_size,
                              hipStream_t stream) {
  using namespace mk;
  Params P;
  P.x = (const u16*)d_in[0]; P.mem0 = (const u16*)d_in[1];
  P.Wq = (const u16*)d_in[2]; P.Wk = (const u16*)d_in[3]; P.Wv = (const u16*)d_in[4]; P.Wo = (const u16*)d_in[5];
  P.wg = (const u16*)d_in[8]; P.wu = (const u16*)d_in[9]; P.wd = (const u16*)d_in[10];
  P.c = (const u16*)d_in[11]; P.wmod = (const u16*)d_in[12];
  P.out = (u16*)d_out; P.ws = (char*)d_ws;
  constexpr int LDS_BYTES = 163840;
  (void)hipFuncSetAttribute((const void*)mega_kernel, hipFuncAttributeMaxDynamicSharedMemorySize, LDS_BYTES);
  hipLaunchKernelGGL(mega_kernel, dim3(256), dim3(256), LDS_BYTES, stream, P);
}
```
